```python
import torch

def _softmax(x, axis=-1):
    return torch.softmax(x, dim=axis)
def _gelu(x):
    return torch.nn.functional.gelu(x, approximate="tanh")
def _silu(x):
    return torch.nn.functional.silu(x)
def _sigmoid(x):
    return torch.sigmoid(x)
def _layernorm(x, axis=-1, eps=1e-5):
    m = x.mean(dim=axis, keepdim=True)
    v = x.var(dim=axis, keepdim=True, unbiased=False)
    return (x - m) / torch.sqrt(v + eps)
def _scatter_add(tgt, idx, src, axis=0):
    return tgt.clone().index_add_(axis, idx, src)
def _segment_sum(src, seg, n):
    out = torch.zeros((n,) + src.shape[1:], dtype=src.dtype)
    return out.index_add_(0, seg, src)
def _pad(x, pw):
    flat = tuple(v for pair in reversed(pw) for v in pair)
    return torch.nn.functional.pad(x, flat)

kernel_name = 'standalone__gqa_block_cached__decode'

def setup_inputs():

    _w = (lambda a: (lambda t: t.to(torch.bfloat16) if t.is_floating_point() else t)(torch.as_tensor(a)))
    B, Sq, D = 2, 1, 1024
    Hq, Hkv, Sk, Dh = 32, 4, 32768, 128
    assert Hq % Hkv == 0, (Hq, Hkv)
    return {
        "x": _w((lambda *s: torch.randn(*s, dtype=torch.bfloat16) * 0.8)(B, Sq, D)),
        "k_cache": _w((lambda *s: torch.randn(*s, dtype=torch.bfloat16) * 0.8)(B, Hkv, Sk, Dh)),
        "v_cache": _w((lambda *s: torch.randn(*s, dtype=torch.bfloat16) * 0.05)(B, Hkv, Sk, Dh)),
        "wq": _w((lambda *s: torch.randn(*s, dtype=torch.bfloat16) * 0.8)(D, Hq * Dh) * (D ** -0.5)),
        "wo": _w((lambda *s: torch.randn(*s, dtype=torch.bfloat16) * 0.05)(Hq * Dh, D) * ((Hq * Dh) ** -0.5)),
    }

def reference(x, k_cache, v_cache, wq, wo):
    B, Sq, D = x.shape
    Hq, Dh = 32, 128
    Hkv = k_cache.shape[1]
    rep = Hq // Hkv
    q = (lambda x, *d: x.permute(*d))((x @ wq).reshape(B, Sq, Hq, Dh), 0, 2, 1, 3)
    k = (lambda x, n, axis: torch.repeat_interleave(x, n, dim=axis))(k_cache, rep, axis=1)
    v = (lambda x, n, axis: torch.repeat_interleave(x, n, dim=axis))(v_cache, rep, axis=1)
    scores = torch.einsum("bhqd,bhkd->bhqk", q, k) * (Dh ** -0.5)
    p = _softmax(scores, axis=-1)
    o = torch.einsum("bhqk,bhkd->bhqd", p, v)
    return (lambda x, *d: x.permute(*d))(o, 0, 2, 1, 3).reshape(B, Sq, Hq * Dh) @ wo
```

```cpp
#include <hip/hip_runtime.h>
#include <hip/hip_bf16.h>
#include <stdint.h>

typedef unsigned v4u __attribute__((ext_vector_type(4)));
typedef unsigned v2u __attribute__((ext_vector_type(2)));
typedef float f32x4 __attribute__((ext_vector_type(4)));
typedef short bf16x8 __attribute__((ext_vector_type(8)));
typedef __bf16 bf16x2 __attribute__((ext_vector_type(2)));
typedef unsigned short u16;

#define KV_AUX 2
#define AUX_SC1 16

constexpr int SK = 32768, DH = 128;
constexpr int NWG = 256, NT = 512;
constexpr unsigned SENT = 0xFFFFFFFFu;
constexpr unsigned SPIN_CAP = 1u << 16;

constexpr unsigned WS_Q = 0;
constexpr unsigned WS_ML = 16384;
constexpr unsigned WS_PO = 32768;
constexpr unsigned WS_F = WS_PO + (1u << 20);
constexpr unsigned WS_END = WS_F + (2u << 20);

constexpr int RED_STRIDE = 132;
constexpr int L_RED = 0;
constexpr int L_Q = L_RED + 8 * 8 * RED_STRIDE;
constexpr int L_QR = L_Q + 512;
constexpr int L_MAX = L_QR + 256;
constexpr int L_SUM = L_MAX + 64;
constexpr int L_O = L_SUM + 64;
constexpr int L_FIN = L_O + 32;
constexpr int L_TOTAL = L_FIN + 64;

template <int CTRL> __device__ __forceinline__ float dpp(float x) {
  return __builtin_bit_cast(float, __builtin_amdgcn_mov_dpp(__builtin_bit_cast(int, x), CTRL, 0xf, 0xf, true));
}
constexpr int ROR1 = 0x121, ROR2 = 0x122, ROR4 = 0x124, ROR8 = 0x128;
__device__ __forceinline__ float x16_32_sum(float x) {
  auto s = __builtin_amdgcn_permlane16_swap(__float_as_uint(x), __float_as_uint(x), false, false);
  x = __uint_as_float(s[0]) + __uint_as_float(s[1]);
  auto t = __builtin_amdgcn_permlane32_swap(__float_as_uint(x), __float_as_uint(x), false, false);
  return __uint_as_float(t[0]) + __uint_as_float(t[1]);
}
__device__ __forceinline__ float x16_32_max(float x) {
  auto s = __builtin_amdgcn_permlane16_swap(__float_as_uint(x), __float_as_uint(x), false, false);
  x = fmaxf(__uint_as_float(s[0]), __uint_as_float(s[1]));
  auto t = __builtin_amdgcn_permlane32_swap(__float_as_uint(x), __float_as_uint(x), false, false);
  return fmaxf(__uint_as_float(t[0]), __uint_as_float(t[1]));
}
__device__ __forceinline__ float par_sum(float x) {
  x += dpp<ROR2>(x); x += dpp<ROR4>(x); x += dpp<ROR8>(x); return x16_32_sum(x);
}
__device__ __forceinline__ float par_max(float x) {
  x = fmaxf(x, dpp<ROR2>(x)); x = fmaxf(x, dpp<ROR4>(x)); x = fmaxf(x, dpp<ROR8>(x)); return x16_32_max(x);
}
__device__ __forceinline__ float dot2(unsigned a, unsigned b, float c) {
  return __builtin_amdgcn_fdot2_f32_bf16(__builtin_bit_cast(bf16x2, a), __builtin_bit_cast(bf16x2, b), c, false);
}
__device__ __forceinline__ unsigned cvtpk(float lo, float hi) {
  unsigned r; asm("v_cvt_pk_bf16_f32 %0, %1, %2" : "=v"(r) : "v"(lo), "v"(hi)); return r;
}
__device__ __forceinline__ f32x4 mfma(v4u a, v4u b, f32x4 c) {
  return __builtin_amdgcn_mfma_f32_16x16x32_bf16(__builtin_bit_cast(bf16x8, a), __builtin_bit_cast(bf16x8, b), c, 0, 0, 0);
}
__device__ __forceinline__ unsigned nosent(float x) { return min(__float_as_uint(x), 0xFFFFFFFEu); }
__device__ __forceinline__ bool ok4(v4u v) { return v.x != SENT && v.y != SENT && v.z != SENT && v.w != SENT; }
#define SB() __builtin_amdgcn_sched_barrier(0)
#define LDS_BARRIER() do { asm volatile("s_waitcnt lgkmcnt(0)" ::: "memory"); __builtin_amdgcn_s_barrier(); asm volatile("" ::: "memory"); } while (0)
#define STAMP(i) do { } while (0)

__global__ void __launch_bounds__(NT, 2)
gqa_decode_fused(const u16* __restrict__ x, const u16* __restrict__ kc, const u16* __restrict__ vc,
                 const u16* __restrict__ wq, const u16* __restrict__ wo, u16* __restrict__ out, unsigned char* ws) {
  __shared__ __attribute__((aligned(16))) float lds[L_TOTAL];
  const int tid = threadIdx.x, lane = tid & 63, wv = __builtin_amdgcn_readfirstlane(tid >> 6);
  const int wg = blockIdx.x, xcd = wg & 7, G = xcd >> 1, idx = (xcd & 1) * 32 + (wg >> 3);
  const int b = idx >> 5, sp = idx & 31;
  const int g = lane >> 4, c = lane & 15;
  STAMP(0);

  const size_t kvoff = ((size_t)(b * 4 + G) * SK + sp * 1024 + wv * 128) * DH;
  const __amdgpu_buffer_rsrc_t kR = __builtin_amdgcn_make_buffer_rsrc((void*)(kc + kvoff), 0, 32768, 0x00020000);
  const __amdgpu_buffer_rsrc_t vR = __builtin_amdgcn_make_buffer_rsrc((void*)(vc + kvoff), 0, 32768, 0x00020000);
  const __amdgpu_buffer_rsrc_t wqR = __builtin_amdgcn_make_buffer_rsrc((void*)(wq + (size_t)wv * 128 * 4096 + G * 1024 + idx * 16), 0, 127 * 8192 + 32, 0x00020000);
  const __amdgpu_buffer_rsrc_t woR = __builtin_amdgcn_make_buffer_rsrc((void*)(wo + (size_t)(G * 1024 + idx * 16) * 1024), 0, 32768, 0x00020000);
  const __amdgpu_buffer_rsrc_t xR = __builtin_amdgcn_make_buffer_rsrc((void*)x, 0, 4096, 0x00020000);
  const __amdgpu_buffer_rsrc_t wsR = __builtin_amdgcn_make_buffer_rsrc((void*)ws, 0, WS_END, 0x00020000);

  const int rg = lane >> 1, chf = lane & 1;
  v2u xv[2];
  xv[0] = __builtin_amdgcn_raw_buffer_load_b64(xR, (wv * 128 + 4 * rg) * 2, 0, 0);
  xv[1] = __builtin_amdgcn_raw_buffer_load_b64(xR, 2048 + (wv * 128 + 4 * rg) * 2, 0, 0);
  v4u wqv[4];
#pragma unroll
  for (int i = 0; i < 4; ++i) wqv[i] = __builtin_amdgcn_raw_buffer_load_b128(wqR, rg * 32768 + chf * 16, i * 8192, 0);
  SB();
  v4u K[8][4];
  v4u V[4][8];
  const int voK = (4 * (c & 3) + (c >> 2)) * 256 + g * 16;
  const int voV = lane * 16;
#define LOADK(st) do { _Pragma("unroll") for (int ks = 0; ks < 4; ++ks) K[st][ks] = __builtin_amdgcn_raw_buffer_load_b128(kR, voK + ks * 64, (st) * 4096, KV_AUX); } while (0)
#define LOADV(T) do { _Pragma("unroll") for (int j = 0; j < 8; ++j) V[T][j] = __builtin_amdgcn_raw_buffer_load_b128(vR, voV + (j & 3) * 1024, (T) * 8192 + (j >> 2) * 4096, KV_AUX); } while (0)
  LOADK(0); LOADK(1);
  SB();
  {
    float qa[2][8];
#pragma unroll
    for (int e = 0; e < 8; ++e) {
      const unsigned sel = (e & 1) ? 0x07060302u : 0x05040100u;
      const unsigned p01 = __builtin_amdgcn_perm(wqv[1][e >> 1], wqv[0][e >> 1], sel);
      const unsigned p23 = __builtin_amdgcn_perm(wqv[3][e >> 1], wqv[2][e >> 1], sel);
#pragma unroll
      for (int bb = 0; bb < 2; ++bb) qa[bb][e] = par_sum(dot2(p23, xv[bb].y, dot2(p01, xv[bb].x, 0.f)));
    }
    if (lane < 2) {
      float* dst = lds + L_QR + wv * 32 + chf * 8;
#pragma unroll
      for (int bb = 0; bb < 2; ++bb) {
        *(f32x4*)(dst + bb * 16) = f32x4{qa[bb][0], qa[bb][1], qa[bb][2], qa[bb][3]};
        *(f32x4*)(dst + bb * 16 + 4) = f32x4{qa[bb][4], qa[bb][5], qa[bb][6], qa[bb][7]};
      }
    }
    LDS_BARRIER();
    if (tid < 16) {
      float s0 = 0.f, s1 = 0.f;
#pragma unroll
      for (int w = 0; w < 8; ++w) { s0 += lds[L_QR + w * 32 + 2 * tid]; s1 += lds[L_QR + w * 32 + 2 * tid + 1]; }
      const unsigned pk = min(cvtpk(s0, s1), 0xFFFFFFFEu);
      __builtin_amdgcn_raw_buffer_store_b32(pk, wsR, WS_Q + (tid >> 3) * 8192 + (G * 1024 + idx * 16) * 2 + (tid & 7) * 4, 0, AUX_SC1);
    }
  }
  STAMP(1);
  SB();
  LOADK(2); LOADK(3); LOADK(4); LOADK(5); LOADK(6); LOADK(7);
  SB();
  const int qoff = WS_Q + b * 8192 + G * 2048 + (tid & 127) * 16;
  v4u qv = __builtin_amdgcn_raw_buffer_load_b128(wsR, qoff, 0, AUX_SC1);
  SB();
  LOADV(0); LOADV(1);
  SB();
  if (wv < 2) {
    unsigned spins = 0;
    while (!__all(ok4(qv))) {
      if (++spins > SPIN_CAP) break;
      __builtin_amdgcn_s_sleep(2);
      qv = __builtin_amdgcn_raw_buffer_load_b128(wsR, qoff, 0, AUX_SC1);
    }
    *(v4u*)(lds + L_Q + tid * 4) = qv;
  }
  LDS_BARRIER();
  STAMP(2);
  v4u Q[4];
#pragma unroll
  for (int ks = 0; ks < 4; ++ks) Q[ks] = *(const v4u*)(lds + L_Q + (c & 7) * 64 + ks * 16 + g * 4);
  f32x4 S[8];
#define QK(st) do { f32x4 a_ = {0.f, 0.f, 0.f, 0.f}; _Pragma("unroll") for (int ks = 0; ks < 4; ++ks) a_ = mfma(K[st][ks], Q[ks], a_); S[st] = a_; } while (0)
  QK(0); QK(1); QK(2); QK(3);
  SB();
  LOADV(2);
  SB();
  QK(4); QK(5); QK(6); QK(7);
  SB();
  LOADV(3);
  unsigned wov[16];
#pragma unroll
  for (int r = 0; r < 16; ++r) wov[r] = __builtin_amdgcn_raw_buffer_load_b32(woR, tid * 4 + (r & 1) * 2048, (r >> 1) * 4096, KV_AUX);
  SB();
  constexpr float C2 = 0.08838834764831845f * 1.4426950408889634f;
  float mx = fmaxf(fmaxf(S[0][0], S[0][1]), fmaxf(S[0][2], S[0][3]));
#pragma unroll
  for (int st = 1; st < 8; ++st) mx = fmaxf(mx, fmaxf(fmaxf(S[st][0], S[st][1]), fmaxf(S[st][2], S[st][3])));
  mx = x16_32_max(mx);
  if (lane < 8) lds[L_MAX + lane * 8 + wv] = mx;
  LDS_BARRIER();
  STAMP(3);
  float M;
  {
    const f32x4 m0 = *(const f32x4*)(lds + L_MAX + (c & 7) * 8), m1 = *(const f32x4*)(lds + L_MAX + (c & 7) * 8 + 4);
    M = fmaxf(fmaxf(fmaxf(m0[0], m0[1]), fmaxf(m0[2], m0[3])), fmaxf(fmaxf(m1[0], m1[1]), fmaxf(m1[2], m1[3]))) * C2;
  }
  const float msub = (M == -INFINITY) ? 0.f : M;
  float lsum = 0.f;
  v4u P[4];
#pragma unroll
  for (int T = 0; T < 4; ++T) {
    float p[8];
#pragma unroll
    for (int j = 0; j < 8; ++j) { p[j] = __builtin_amdgcn_exp2f(fmaf(S[2 * T + (j >> 2)][j & 3], C2, -msub)); lsum += p[j]; }
    P[T] = v4u{cvtpk(p[0], p[1]), cvtpk(p[2], p[3]), cvtpk(p[4], p[5]), cvtpk(p[6], p[7])};
  }
  lsum = x16_32_sum(lsum);
  if (lane < 8) lds[L_SUM + lane * 8 + wv] = lsum;
  f32x4 O[8];
#pragma unroll
  for (int e = 0; e < 8; ++e) O[e] = f32x4{0.f, 0.f, 0.f, 0.f};
#pragma unroll
  for (int T = 0; T < 4; ++T) {
#pragma unroll
    for (int e = 0; e < 8; ++e) {
      const unsigned sel = (e & 1) ? 0x07060302u : 0x05040100u;
      v4u a;
#pragma unroll
      for (int d = 0; d < 4; ++d) a[d] = __builtin_amdgcn_perm(V[T][2 * d + 1][e >> 1], V[T][2 * d][e >> 1], sel);
      O[e] = mfma(a, P[T], O[e]);
    }
    SB();
  }
  STAMP(4);
  if (c < 8) {
    float* dst = lds + L_RED + (wv * 8 + c) * RED_STRIDE + 32 * g;
#pragma unroll
    for (int i = 0; i < 4; ++i) {
      *(f32x4*)(dst + 8 * i) = f32x4{O[0][i], O[1][i], O[2][i], O[3][i]};
      *(f32x4*)(dst + 8 * i + 4) = f32x4{O[4][i], O[5][i], O[6][i], O[7][i]};
    }
  }
  LDS_BARRIER();
  if (tid < 256) {
    const int h = tid >> 5, d4 = tid & 31;
    f32x4 a = *(const f32x4*)(lds + L_RED + h * RED_STRIDE + 4 * d4);
#pragma unroll
    for (int w = 1; w < 8; ++w) a += *(const f32x4*)(lds + L_RED + (w * 8 + h) * RED_STRIDE + 4 * d4);
    const int dc = d4 >> 2, half = (d4 >> 1) & 1, sub = d4 & 1;
    const unsigned off = WS_PO + ((((((G * 8 + h) * 8 + dc) * 2 + b) * 2 + half) * 32 + sp) * 2 + sub) * 16;
    __builtin_amdgcn_raw_buffer_store_b128(v4u{nosent(a[0]), nosent(a[1]), nosent(a[2]), nosent(a[3])}, wsR, off, 0, AUX_SC1);
  } else if (tid < 264) {
    const int h = lane;
    const f32x4 l0 = *(const f32x4*)(lds + L_SUM + h * 8), l1 = *(const f32x4*)(lds + L_SUM + h * 8 + 4);
    const float L = ((l0[0] + l0[1]) + (l0[2] + l0[3])) + ((l1[0] + l1[1]) + (l1[2] + l1[3]));
    const unsigned off = WS_ML + (((G * 8 + h) * 2 + b) * 32 + sp) * 8;
    __builtin_amdgcn_raw_buffer_store_b64(v2u{nosent(M), nosent(L)}, wsR, off, 0, AUX_SC1);
  }
  STAMP(5);
  if (wv < 4) {
    const int bb = wv >> 1, half = wv & 1, h = idx >> 3, dc = idx & 7;
    const unsigned offp = WS_PO + ((((G * 8 + h) * 8 + dc) * 2 + bb) * 2 + half) * 1024 + lane * 16;
    const unsigned offm = WS_ML + (((G * 8 + h) * 2 + bb) * 32 + (lane >> 1)) * 8;
    v4u pa; v2u ml;
    unsigned spins = 0;
    for (;;) {
      pa = __builtin_amdgcn_raw_buffer_load_b128(wsR, offp, 0, AUX_SC1);
      ml = __builtin_amdgcn_raw_buffer_load_b64(wsR, offm, 0, AUX_SC1);
      if (__all(ok4(pa) && ml.x != SENT && ml.y != SENT)) break;
      if (++spins > SPIN_CAP) break;
      __builtin_amdgcn_s_sleep(1);
    }
    const float ms = __uint_as_float(ml.x), ls = __uint_as_float(ml.y);
    const float Mg = par_max(ms);
    const float wgt = (ls > 0.f) ? __builtin_amdgcn_exp2f(ms - Mg) : 0.f;
    const float Lt = par_sum(wgt * ls);
    const float inv = 1.f / Lt;
    f32x4 o;
#pragma unroll
    for (int i = 0; i < 4; ++i) o[i] = par_sum(wgt * __uint_as_float(pa[i])) * inv;
    if (lane < 2) *(f32x4*)(lds + L_O + bb * 16 + half * 8 + lane * 4) = o;
  }
  LDS_BARRIER();
  STAMP(6);
  {
    float r00 = 0.f, r01 = 0.f, r10 = 0.f, r11 = 0.f;
#pragma unroll
    for (int r4 = 0; r4 < 4; ++r4) {
      const f32x4 o0 = *(const f32x4*)(lds + L_O + r4 * 4), o1 = *(const f32x4*)(lds + L_O + 16 + r4 * 4);
#pragma unroll
      for (int i = 0; i < 4; ++i) {
        const unsigned w2 = wov[r4 * 4 + i];
        const float w0 = __uint_as_float(w2 << 16), w1 = __uint_as_float(w2 & 0xffff0000u);
        r00 = fmaf(o0[i], w0, r00); r01 = fmaf(o0[i], w1, r01);
        r10 = fmaf(o1[i], w0, r10); r11 = fmaf(o1[i], w1, r11);
      }
    }
    const unsigned off = WS_F + (((tid >> 1) * 256 + wg) * 2 + (tid & 1)) * 16;
    __builtin_amdgcn_raw_buffer_store_b128(v4u{nosent(r00), nosent(r01), nosent(r10), nosent(r11)}, wsR, off, 0, AUX_SC1);
  }
  STAMP(7);
  {
    const unsigned off = WS_F + wg * 8192 + tid * 16;
    v4u f;
    unsigned spins = 0;
    for (;;) {
      f = __builtin_amdgcn_raw_buffer_load_b128(wsR, off, 0, AUX_SC1);
      if (__all(ok4(f))) break;
      if (++spins > SPIN_CAP) break;
      __builtin_amdgcn_s_sleep(1);
    }
    f32x4 s;
#pragma unroll
    for (int i = 0; i < 4; ++i) s[i] = par_sum(__uint_as_float(f[i]));
    if (lane < 2) *(f32x4*)(lds + L_FIN + (wv * 2 + lane) * 4) = s;
    LDS_BARRIER();
    if (tid < 4) {
      const int bb = tid >> 1, sub = tid & 1;
      float s0 = 0.f, s1 = 0.f;
#pragma unroll
      for (int w = 0; w < 8; ++w) { s0 += lds[L_FIN + (w * 2 + sub) * 4 + bb * 2]; s1 += lds[L_FIN + (w * 2 + sub) * 4 + bb * 2 + 1]; }
      *(unsigned*)(out + bb * 1024 + wg * 4 + sub * 2) = cvtpk(s0, s1);
    }
  }
  STAMP(8);
}

extern "C" void kernel_launch(void* const* d_in, const int* in_sizes, int n_in,
                              void* d_out, int out_size, void* d_ws, size_t ws_size,
                              hipStream_t stream) {
  (void)in_sizes; (void)n_in; (void)out_size; (void)ws_size;
  (void)hipMemsetAsync(d_ws, 0xFF, WS_END, stream);
  gqa_decode_fused<<<NWG, NT, 0, stream>>>((const u16*)d_in[0], (const u16*)d_in[1], (const u16*)d_in[2],
                                           (const u16*)d_in[3], (const u16*)d_in[4], (u16*)d_out, (unsigned char*)d_ws);
}
```

```cpp
#include <hip/hip_runtime.h>
#include <hip/hip_bf16.h>
#include <stdint.h>

typedef unsigned v4u __attribute__((ext_vector_type(4)));
typedef unsigned v2u __attribute__((ext_vector_type(2)));
typedef float f32x4 __attribute__((ext_vector_type(4)));
typedef short bf16x8 __attribute__((ext_vector_type(8)));
typedef __bf16 bf16x2 __attribute__((ext_vector_type(2)));
typedef unsigned short u16;

#define AUX_NT 2
#define AUX_SC1 16
#define KMAP 0
#define PACE 16
#define FLAY 0
#define PLAY 0

constexpr int SK = 32768, DH = 128;
constexpr int NWG = 256, NT = 512;
constexpr unsigned SENT = 0xFFFFFFFFu;
constexpr unsigned SPIN_CAP = 1u << 16;

constexpr unsigned WS_EPOCH = 0;
constexpr unsigned WS_Q = 4096;
constexpr unsigned WS_ML = WS_Q + 65536;
constexpr unsigned WS_PO = WS_ML + 16384;
constexpr unsigned WS_F = WS_PO + (1u << 20);
constexpr unsigned WS_END = WS_F + (2u << 20);

constexpr int RED_STRIDE = 132;
constexpr int L_RED = 0;
constexpr int L_Q = L_RED + 8 * 8 * RED_STRIDE;
constexpr int L_QR = L_Q + 512;
constexpr int L_MAX = L_QR + 256;
constexpr int L_SUM = L_MAX + 64;
constexpr int L_O = L_SUM + 64;
constexpr int L_FIN = L_O + 32;
constexpr int L_FLAG = L_FIN + 64;
constexpr int L_TOTAL = L_FLAG + 4;

template <int CTRL> __device__ __forceinline__ float dpp(float x) {
  return __builtin_bit_cast(float, __builtin_amdgcn_mov_dpp(__builtin_bit_cast(int, x), CTRL, 0xf, 0xf, true));
}
constexpr int ROR2 = 0x122, ROR4 = 0x124, ROR8 = 0x128;
__device__ __forceinline__ float x16_32_sum(float x) {
  auto s = __builtin_amdgcn_permlane16_swap(__float_as_uint(x), __float_as_uint(x), false, false);
  x = __uint_as_float(s[0]) + __uint_as_float(s[1]);
  auto t = __builtin_amdgcn_permlane32_swap(__float_as_uint(x), __float_as_uint(x), false, false);
  return __uint_as_float(t[0]) + __uint_as_float(t[1]);
}
__device__ __forceinline__ float x16_32_max(float x) {
  auto s = __builtin_amdgcn_permlane16_swap(__float_as_uint(x), __float_as_uint(x), false, false);
  x = fmaxf(__uint_as_float(s[0]), __uint_as_float(s[1]));
  auto t = __builtin_amdgcn_permlane32_swap(__float_as_uint(x), __float_as_uint(x), false, false);
  return fmaxf(__uint_as_float(t[0]), __uint_as_float(t[1]));
}
__device__ __forceinline__ float par_sum(float x) {
  x += dpp<ROR2>(x); x += dpp<ROR4>(x); x += dpp<ROR8>(x); return x16_32_sum(x);
}
__device__ __forceinline__ float par_max(float x) {
  x = fmaxf(x, dpp<ROR2>(x)); x = fmaxf(x, dpp<ROR4>(x)); x = fmaxf(x, dpp<ROR8>(x)); return x16_32_max(x);
}
__device__ __forceinline__ float dot2(unsigned a, unsigned b, float c) {
  return __builtin_amdgcn_fdot2_f32_bf16(__builtin_bit_cast(bf16x2, a), __builtin_bit_cast(bf16x2, b), c, false);
}
__device__ __forceinline__ unsigned cvtpk(float lo, float hi) {
  unsigned r; asm("v_cvt_pk_bf16_f32 %0, %1, %2" : "=v"(r) : "v"(lo), "v"(hi)); return r;
}
__device__ __forceinline__ f32x4 mfma(v4u a, v4u b, f32x4 c) {
  return __builtin_amdgcn_mfma_f32_16x16x32_bf16(__builtin_bit_cast(bf16x8, a), __builtin_bit_cast(bf16x8, b), c, 0, 0, 0);
}
__device__ __forceinline__ unsigned nosent(float x) { return min(__float_as_uint(x), 0xFFFFFFFEu); }
__device__ __forceinline__ bool ok4(v4u v) { return max(max(v.x, v.y), max(v.z, v.w)) != SENT; }
typedef __attribute__((address_space(3))) unsigned lds_u32;
__device__ __forceinline__ void lds_flag_store(float* p, unsigned v) { __hip_atomic_store((lds_u32*)p, v, __ATOMIC_RELAXED, __HIP_MEMORY_SCOPE_WORKGROUP); }
__device__ __forceinline__ unsigned lds_flag_load(float* p) { return __hip_atomic_load((lds_u32*)p, __ATOMIC_RELAXED, __HIP_MEMORY_SCOPE_WORKGROUP); }
__device__ __forceinline__ unsigned tagdiff(v4u v, unsigned lo, unsigned hi) { return ((v.x >> 16) ^ lo) | (v.y ^ hi) | ((v.z >> 16) ^ lo) | (v.w ^ hi); }
#define SB() __builtin_amdgcn_sched_barrier(0)
#define LDS_BARRIER() do { asm volatile("s_waitcnt lgkmcnt(0)" ::: "memory"); __builtin_amdgcn_s_barrier(); asm volatile("" ::: "memory"); } while (0)
#define STR2(x) #x
#define STR(x) STR2(x)
#define VMWAIT(n) asm volatile("s_waitcnt vmcnt(" STR(n) ")" ::: "memory")
#define STAMP(i) do { } while (0)

__global__ void __launch_bounds__(NT, 2)
gqa_decode_fused(const u16* __restrict__ x, const u16* __restrict__ kc, const u16* __restrict__ vc,
                 const u16* __restrict__ wq, const u16* __restrict__ wo, u16* __restrict__ out, unsigned char* ws) {
  __shared__ __attribute__((aligned(16))) float lds[L_TOTAL];
  const int tid = threadIdx.x, lane = tid & 63, wv = __builtin_amdgcn_readfirstlane(tid >> 6);
  const int wg = blockIdx.x, xcd = wg & 7, G = xcd >> 1, b = xcd & 1, sp = wg >> 3, idx = b * 32 + sp;
  const int g = lane >> 4, c = lane & 15;
  STAMP(0);

  constexpr unsigned US = KMAP == 0 ? (2u << 20) : KMAP == 1 ? 65536u : 8192u;
  const unsigned wbase = KMAP == 0 ? (sp * 8 + wv) * 8192u : KMAP == 1 ? sp * 262144u + wv * 8192u : sp * 262144u + wv * 32768u;
  const size_t kvoff = (size_t)(b * 4 + G) * SK * DH * 2 + wbase;
  const __amdgpu_buffer_rsrc_t kR = __builtin_amdgcn_make_buffer_rsrc((void*)((const unsigned char*)kc + kvoff), 0, 3 * US + 8192, 0x00020000);
  const __amdgpu_buffer_rsrc_t vR = __builtin_amdgcn_make_buffer_rsrc((void*)((const unsigned char*)vc + kvoff), 0, 3 * US + 8192, 0x00020000);
  const __amdgpu_buffer_rsrc_t wqR = __builtin_amdgcn_make_buffer_rsrc((void*)(wq + (size_t)wv * 128 * 4096 + G * 1024 + idx * 16), 0, 127 * 8192 + 32, 0x00020000);
  const __amdgpu_buffer_rsrc_t woR = __builtin_amdgcn_make_buffer_rsrc((void*)(wo + (size_t)(G * 1024 + idx * 16) * 1024), 0, 32768, 0x00020000);
  const __amdgpu_buffer_rsrc_t xR = __builtin_amdgcn_make_buffer_rsrc((void*)x, 0, 4096, 0x00020000);
  const __amdgpu_buffer_rsrc_t wsR = __builtin_amdgcn_make_buffer_rsrc((void*)ws, 0, WS_END, 0x00020000);

  const int rg = lane >> 1, chf = lane & 1;
  v2u xv[2];
  xv[0] = __builtin_amdgcn_raw_buffer_load_b64(xR, (wv * 128 + 4 * rg) * 2, 0, AUX_NT);
  xv[1] = __builtin_amdgcn_raw_buffer_load_b64(xR, 2048 + (wv * 128 + 4 * rg) * 2, 0, AUX_NT);
  v4u wqv[4];
#pragma unroll
  for (int i = 0; i < 4; ++i) wqv[i] = __builtin_amdgcn_raw_buffer_load_b128(wqR, rg * 32768 + chf * 16, i * 8192, AUX_NT);
  const v2u ep = __builtin_amdgcn_raw_buffer_load_b64(wsR, WS_EPOCH, 0, AUX_SC1);
  unsigned off_po, off_f;
  {
    const int h = (tid >> 5) & 7, d4 = tid & 31;
    if (PLAY == 0) off_po = WS_PO + ((G * 2 + b) * 32 + sp) * 4096 + (tid & 255) * 16;
    else off_po = WS_PO + ((((((G * 8 + h) * 8 + (d4 >> 2)) * 2 + b) * 2 + ((d4 >> 1) & 1)) * 32 + sp) * 2 + (d4 & 1)) * 16;
    if (tid >= 256) off_po = WS_ML + (((G * 8 + (tid & 7)) * 2 + b) * 32 + sp) * 8;
    if (FLAY == 0) off_f = WS_F + wg * 8192 + tid * 16;
    else off_f = WS_F + (((tid >> 1) * 256 + wg) * 2 + (tid & 1)) * 16;
  }
  __builtin_amdgcn_raw_buffer_store_b128(v4u{SENT, SENT, SENT, SENT}, wsR, off_f, 0, AUX_SC1);
  if (tid < 256) __builtin_amdgcn_raw_buffer_store_b128(v4u{SENT, SENT, SENT, SENT}, wsR, off_po, 0, AUX_SC1);
  else if (tid < 264) __builtin_amdgcn_raw_buffer_store_b64(v2u{SENT, SENT}, wsR, off_po, 0, AUX_SC1);
  if (tid == 0) lds_flag_store(lds + L_FLAG, 0u);
  SB();
  v4u K[4][2][4];
  v4u V[4][8];
  const int voK = (4 * (c & 3) + ((c >> 2) & 1)) * 256 + (c >> 3) * 64 + g * 16;
  const int voV = lane * 16;
#define LOADK(T, t) do { _Pragma("unroll") for (int p_ = 0; p_ < 4; ++p_) K[T][t][p_] = __builtin_amdgcn_raw_buffer_load_b128(kR, voK + (p_ & 1) * 128 + (p_ >> 1) * 512, (T) * US + (t) * 4096, AUX_NT); SB(); } while (0)
#define LOADV(T, t) do { _Pragma("unroll") for (int j_ = 0; j_ < 4; ++j_) V[T][(t) * 4 + j_] = __builtin_amdgcn_raw_buffer_load_b128(vR, voV + j_ * 1024, (T) * US + (t) * 4096, AUX_NT); SB(); } while (0)
#define PACEW() do { VMWAIT(PACE); SB(); } while (0)
  LOADK(0, 0); LOADK(0, 1); LOADK(1, 0); LOADK(1, 1);
  const unsigned long long tag = ((((unsigned long long)ep.y << 32) | ep.x) + 1ull) & 0xFFFFFFFFFFFFull;
  const unsigned tag_lo = (unsigned)tag & 0xFFFFu, tag_hi = (unsigned)(tag >> 16);
  {
    float qa[2][8];
#pragma unroll
    for (int e = 0; e < 8; ++e) {
      const unsigned sel = (e & 1) ? 0x07060302u : 0x05040100u;
      const unsigned p01 = __builtin_amdgcn_perm(wqv[1][e >> 1], wqv[0][e >> 1], sel);
      const unsigned p23 = __builtin_amdgcn_perm(wqv[3][e >> 1], wqv[2][e >> 1], sel);
#pragma unroll
      for (int bb = 0; bb < 2; ++bb) qa[bb][e] = par_sum(dot2(p23, xv[bb].y, dot2(p01, xv[bb].x, 0.f)));
    }
    if (lane < 2) {
      float* dst = lds + L_QR + wv * 32 + chf * 8;
#pragma unroll
      for (int bb = 0; bb < 2; ++bb) {
        *(f32x4*)(dst + bb * 16) = f32x4{qa[bb][0], qa[bb][1], qa[bb][2], qa[bb][3]};
        *(f32x4*)(dst + bb * 16 + 4) = f32x4{qa[bb][4], qa[bb][5], qa[bb][6], qa[bb][7]};
      }
    }
    VMWAIT(16);
    LDS_BARRIER();
    if (tid < 32) {
      float s0 = 0.f;
#pragma unroll
      for (int w = 0; w < 8; ++w) s0 += lds[L_QR + w * 32 + tid];
      const unsigned pk = cvtpk(s0, s0) & 0xFFFFu;
      __builtin_amdgcn_raw_buffer_store_b64(v2u{pk | (tag_lo << 16), tag_hi}, wsR, WS_Q + ((tid >> 4) * 4096 + G * 1024 + idx * 16 + (tid & 15)) * 8, 0, AUX_SC1);
    }
  }
  STAMP(1);
  SB();
  PACEW(); LOADK(2, 0); PACEW(); LOADK(2, 1);
  if (wv == 0) {
    v4u qg[8];
    const int qoff = WS_Q + (b * 4096 + G * 1024) * 8 + lane * 16;
#pragma unroll
    for (int k = 0; k < 8; ++k) qg[k] = __builtin_amdgcn_raw_buffer_load_b128(wsR, qoff + (k & 3) * 1024, (k >> 2) * 4096, AUX_SC1);
    SB();
    LOADK(3, 0); LOADK(3, 1);
    unsigned bad = 0;
#pragma unroll
    for (int k = 0; k < 8; ++k) bad |= tagdiff(qg[k], tag_lo, tag_hi);
    if (!__all(bad == 0)) {
      unsigned spins = 0;
      do {
        __builtin_amdgcn_s_sleep(4);
        bad = 0;
#pragma unroll
        for (int k = 0; k < 8; ++k) qg[k] = __builtin_amdgcn_raw_buffer_load_b128(wsR, qoff + (k & 3) * 1024, (k >> 2) * 4096, AUX_SC1);
#pragma unroll
        for (int k = 0; k < 8; ++k) bad |= tagdiff(qg[k], tag_lo, tag_hi);
      } while (!__all(bad == 0) && ++spins < SPIN_CAP);
    }
#pragma unroll
    for (int k = 0; k < 8; ++k) ((unsigned*)lds)[L_Q + k * 64 + lane] = (qg[k].x & 0xFFFFu) | (qg[k].z << 16);
    asm volatile("s_waitcnt lgkmcnt(0)" ::: "memory");
    if (lane == 0) lds_flag_store(lds + L_FLAG, 1u);
  } else {
    PACEW(); LOADK(3, 0); PACEW(); LOADK(3, 1);
  }
  STAMP(2);
  PACEW(); LOADV(0, 0); PACEW(); LOADV(0, 1);
  const int hoff = WS_Q + (wv * 32 + (lane & 31)) * 128;
  v2u hg = __builtin_amdgcn_raw_buffer_load_b64(wsR, hoff, 0, AUX_SC1);
  SB();
  PACEW(); LOADV(1, 0); PACEW(); LOADV(1, 1);
  while (lds_flag_load(lds + L_FLAG) == 0u) __builtin_amdgcn_s_sleep(1);
  asm volatile("" ::: "memory");
  v4u Q[2];
#pragma unroll
  for (int h = 0; h < 2; ++h) Q[h] = *(const v4u*)(lds + L_Q + (c & 7) * 64 + h * 32 + (c >> 3) * 16 + g * 4);
  f32x4 S[4][2];
#define QK(T, t) do { const f32x4 z_ = {0.f, 0.f, 0.f, 0.f}; \
    const f32x4 dx_ = mfma(K[T][t][1], Q[1], mfma(K[T][t][0], Q[0], z_)), dy_ = mfma(K[T][t][3], Q[1], mfma(K[T][t][2], Q[0], z_)); \
    _Pragma("unroll") for (int i_ = 0; i_ < 4; ++i_) { \
      auto sw_ = __builtin_amdgcn_permlane32_swap(__float_as_uint(dx_[i_]), __float_as_uint(dy_[i_]), false, false); \
      S[T][t][i_] = __uint_as_float(sw_[0]) + dpp<ROR8>(__uint_as_float(sw_[1])); } } while (0)
  QK(0, 0); QK(0, 1); SB();
  LOADV(2, 0); LOADV(2, 1);
  QK(1, 0); QK(1, 1); SB();
  LOADV(3, 0); LOADV(3, 1);
  unsigned wov[16];
#pragma unroll
  for (int r = 0; r < 16; ++r) wov[r] = __builtin_amdgcn_raw_buffer_load_b32(woR, tid * 4 + (r & 1) * 2048, (r >> 1) * 4096, AUX_NT);
  SB();
  QK(2, 0); QK(2, 1); QK(3, 0); QK(3, 1);
  STAMP(3);
  constexpr float C2 = 0.08838834764831845f * 1.4426950408889634f;
  float mx = -INFINITY;
#pragma unroll
  for (int T = 0; T < 4; ++T)
#pragma unroll
    for (int t = 0; t < 2; ++t) mx = fmaxf(mx, fmaxf(fmaxf(S[T][t][0], S[T][t][1]), fmaxf(S[T][t][2], S[T][t][3])));
  mx = x16_32_max(mx) * C2;
  if (lane < 8) lds[L_MAX + lane * 8 + wv] = mx;
  float lsum = 0.f;
  f32x4 O[8];
#pragma unroll
  for (int e = 0; e < 8; ++e) O[e] = f32x4{0.f, 0.f, 0.f, 0.f};
#define EXPP(T, P_, msub_) do { float p_[8]; \
    _Pragma("unroll") for (int j_ = 0; j_ < 8; ++j_) { p_[j_] = __builtin_amdgcn_exp2f(fmaf(S[T][j_ >> 2][j_ & 3], C2, -(msub_))); lsum += p_[j_]; } \
    P_ = v4u{cvtpk(p_[0], p_[1]), cvtpk(p_[2], p_[3]), cvtpk(p_[4], p_[5]), cvtpk(p_[6], p_[7])}; } while (0)
#define PV(T, P_) do { _Pragma("unroll") for (int e_ = 0; e_ < 8; ++e_) { \
      const unsigned sel_ = (e_ & 1) ? 0x07060302u : 0x05040100u; v4u a_; \
      _Pragma("unroll") for (int d_ = 0; d_ < 4; ++d_) a_[d_] = __builtin_amdgcn_perm(V[T][2 * d_ + 1][e_ >> 1], V[T][2 * d_][e_ >> 1], sel_); \
      O[e_] = mfma(a_, P_, O[e_]); } SB(); } while (0)
  {
    const float msub = (mx == -INFINITY) ? 0.f : mx;
    v4u P0, P1;
    EXPP(0, P0, msub); EXPP(1, P1, msub);
    PV(0, P0); PV(1, P1);
  }
  if (!__all((((hg.x >> 16) ^ tag_lo) | (hg.y ^ tag_hi)) == 0u)) {
    unsigned spins = 0;
    do {
      __builtin_amdgcn_s_sleep(4);
      hg = __builtin_amdgcn_raw_buffer_load_b64(wsR, hoff, 0, AUX_SC1);
    } while (!__all((((hg.x >> 16) ^ tag_lo) | (hg.y ^ tag_hi)) == 0u) && ++spins < SPIN_CAP);
  }
  LDS_BARRIER();
  float M;
  {
    const f32x4 m0 = *(const f32x4*)(lds + L_MAX + (c & 7) * 8), m1 = *(const f32x4*)(lds + L_MAX + (c & 7) * 8 + 4);
    M = fmaxf(fmaxf(fmaxf(m0[0], m0[1]), fmaxf(m0[2], m0[3])), fmaxf(fmaxf(m1[0], m1[1]), fmaxf(m1[2], m1[3])));
    if (c >= 8) M = mx;
    const float msub = (M == -INFINITY) ? 0.f : M;
    const float f = __builtin_amdgcn_exp2f(mx - msub);
    lsum *= f;
#pragma unroll
    for (int e = 0; e < 8; ++e) O[e] *= f;
    v4u P2, P3;
    EXPP(2, P2, msub); EXPP(3, P3, msub);
    lsum = x16_32_sum(lsum);
    if (lane < 8) lds[L_SUM + lane * 8 + wv] = lsum;
    SB();
    PV(2, P2); PV(3, P3);
  }
  STAMP(4);
  if (c < 8) {
    float* dst = lds + L_RED + (wv * 8 + c) * RED_STRIDE + 32 * g;
#pragma unroll
    for (int i = 0; i < 4; ++i) {
      *(f32x4*)(dst + 8 * i) = f32x4{O[0][i], O[1][i], O[2][i], O[3][i]};
      *(f32x4*)(dst + 8 * i + 4) = f32x4{O[4][i], O[5][i], O[6][i], O[7][i]};
    }
  }
  LDS_BARRIER();
  STAMP(5);
  if (tid < 256) {
    const int h = tid >> 5, d4 = tid & 31;
    f32x4 a = *(const f32x4*)(lds + L_RED + h * RED_STRIDE + 4 * d4);
#pragma unroll
    for (int w = 1; w < 8; ++w) a += *(const f32x4*)(lds + L_RED + (w * 8 + h) * RED_STRIDE + 4 * d4);
    __builtin_amdgcn_raw_buffer_store_b128(v4u{nosent(a[0]), nosent(a[1]), nosent(a[2]), nosent(a[3])}, wsR, off_po, 0, AUX_SC1);
  } else if (tid < 264) {
    const f32x4 l0 = *(const f32x4*)(lds + L_SUM + lane * 8), l1 = *(const f32x4*)(lds + L_SUM + lane * 8 + 4);
    const float L = ((l0[0] + l0[1]) + (l0[2] + l0[3])) + ((l1[0] + l1[1]) + (l1[2] + l1[3]));
    __builtin_amdgcn_raw_buffer_store_b64(v2u{nosent(M), nosent(L)}, wsR, off_po, 0, AUX_SC1);
  }
  STAMP(6);
  if (wv < 4) {
    const int bb = wv >> 1, half = wv & 1, h = idx >> 3, dc = idx & 7;
    const unsigned offp = PLAY == 0 ? WS_PO + ((G * 2 + bb) * 32 + (lane >> 1)) * 4096 + h * 512 + dc * 64 + half * 32 + (lane & 1) * 16
                                    : WS_PO + ((((G * 8 + h) * 8 + dc) * 2 + bb) * 2 + half) * 1024 + lane * 16;
    const unsigned offm = WS_ML + (((G * 8 + h) * 2 + bb) * 32 + (lane >> 1)) * 8;
    v4u pa = __builtin_amdgcn_raw_buffer_load_b128(wsR, offp, 0, AUX_SC1);
    v2u ml = __builtin_amdgcn_raw_buffer_load_b64(wsR, offm, 0, AUX_SC1);
    unsigned spins = 0;
    for (;;) {
      const bool ok = ok4(pa) & (max(ml.x, ml.y) != SENT);
      if (__all(ok) || ++spins > SPIN_CAP) break;
      __builtin_amdgcn_s_sleep(1);
      if (!ok) {
        pa = __builtin_amdgcn_raw_buffer_load_b128(wsR, offp, 0, AUX_SC1);
        ml = __builtin_amdgcn_raw_buffer_load_b64(wsR, offm, 0, AUX_SC1);
      }
    }
    const float ms = __uint_as_float(ml.x), ls = __uint_as_float(ml.y);
    const float Mg = par_max(ms);
    const float wgt = (ls > 0.f) ? __builtin_amdgcn_exp2f(ms - Mg) : 0.f;
    const float Lt = par_sum(wgt * ls);
    const float inv = 1.f / Lt;
    f32x4 o;
#pragma unroll
    for (int i = 0; i < 4; ++i) o[i] = par_sum(wgt * __uint_as_float(pa[i])) * inv;
    if (lane < 2) *(f32x4*)(lds + L_O + bb * 16 + half * 8 + lane * 4) = o;
  }
  LDS_BARRIER();
  STAMP(7);
  {
    float r00 = 0.f, r01 = 0.f, r10 = 0.f, r11 = 0.f;
#pragma unroll
    for (int r4 = 0; r4 < 4; ++r4) {
      const f32x4 o0 = *(const f32x4*)(lds + L_O + r4 * 4), o1 = *(const f32x4*)(lds + L_O + 16 + r4 * 4);
#pragma unroll
      for (int i = 0; i < 4; ++i) {
        const unsigned w2 = wov[r4 * 4 + i];
        const float w0 = __uint_as_float(w2 << 16), w1 = __uint_as_float(w2 & 0xffff0000u);
        r00 = fmaf(o0[i], w0, r00); r01 = fmaf(o0[i], w1, r01);
        r10 = fmaf(o1[i], w0, r10); r11 = fmaf(o1[i], w1, r11);
      }
    }
    __builtin_amdgcn_raw_buffer_store_b128(v4u{nosent(r00), nosent(r01), nosent(r10), nosent(r11)}, wsR, off_f, 0, AUX_SC1);
  }
  STAMP(8);
  {
    const unsigned off = FLAY == 0 ? WS_F + (tid >> 1) * 8192 + wg * 32 + (tid & 1) * 16 : WS_F + wg * 8192 + tid * 16;
    v4u f = __builtin_amdgcn_raw_buffer_load_b128(wsR, off, 0, AUX_SC1);
    unsigned spins = 0;
    for (;;) {
      const bool ok = ok4(f);
      if (__all(ok) || ++spins > SPIN_CAP) break;
      __builtin_amdgcn_s_sleep(1);
      if (!ok) f = __builtin_amdgcn_raw_buffer_load_b128(wsR, off, 0, AUX_SC1);
    }
    f32x4 s;
#pragma unroll
    for (int i = 0; i < 4; ++i) s[i] = par_sum(__uint_as_float(f[i]));
    if (lane < 2) *(f32x4*)(lds + L_FIN + (wv * 2 + lane) * 4) = s;
    LDS_BARRIER();
    if (tid < 4) {
      const int bb = tid >> 1, sub = tid & 1;
      float s0 = 0.f, s1 = 0.f;
#pragma unroll
      for (int w = 0; w < 8; ++w) { s0 += lds[L_FIN + (w * 2 + sub) * 4 + bb * 2]; s1 += lds[L_FIN + (w * 2 + sub) * 4 + bb * 2 + 1]; }
      *(unsigned*)(out + bb * 1024 + wg * 4 + sub * 2) = cvtpk(s0, s1);
    }
    if (wg == 0 && tid == 64) __builtin_amdgcn_raw_buffer_store_b64(v2u{(unsigned)tag, (unsigned)(tag >> 32)}, wsR, WS_EPOCH, 0, AUX_SC1);
  }
  STAMP(9);
}

extern "C" void kernel_launch(void* const* d_in, const int* in_sizes, int n_in,
                              void* d_out, int out_size, void* d_ws, size_t ws_size,
                              hipStream_t stream) {
  (void)in_sizes; (void)n_in; (void)out_size; (void)ws_size;
  gqa_decode_fused<<<NWG, NT, 0, stream>>>((const u16*)d_in[0], (const u16*)d_in[1], (const u16*)d_in[2],
                                           (const u16*)d_in[3], (const u16*)d_in[4], (u16*)d_out, (unsigned char*)d_ws);
}
```

```cpp
#include <hip/hip_runtime.h>
#include <hip/hip_bf16.h>
#include <stdint.h>

typedef unsigned v4u __attribute__((ext_vector_type(4)));
typedef unsigned v2u __attribute__((ext_vector_type(2)));
typedef float f32x4 __attribute__((ext_vector_type(4)));
typedef short bf16x8 __attribute__((ext_vector_type(8)));
typedef short v4s __attribute__((ext_vector_type(4)));
typedef __bf16 bf16x2 __attribute__((ext_vector_type(2)));
typedef unsigned short u16;

#define AUX_NT 2
#define AUX_SC1 16
#define POLL_MANY 16
#define POLL_SLEEP 12
#define KMAP 0

constexpr int SK = 32768, DH = 128;
constexpr int NWG = 256, NT = 512;
#define ABL 0
constexpr unsigned SPIN_CAP = ABL ? 1u : 1u << 16;

constexpr unsigned WS_EPOCH = 0;
constexpr unsigned WS_QP = 4096;
constexpr unsigned WS_Q = WS_QP + 262144;
constexpr unsigned WS_ML = WS_Q + 65536;
constexpr unsigned WS_PO = WS_ML + 65536;
constexpr unsigned WS_F = WS_PO + (1u << 20);
constexpr unsigned WS_FL = WS_F + (1u << 20);
constexpr unsigned WS_END = WS_FL + 32768;

constexpr int RED_STRIDE = 132, WOT_STRIDE = 36;
constexpr int L_RED = 0;
constexpr int L_WOT = L_RED + 8 * 8 * RED_STRIDE;
constexpr int L_Q = L_WOT + 8 * 32 * WOT_STRIDE;
constexpr int L_QR = L_Q + 512;
constexpr int L_MAX = L_QR + 1024;
constexpr int L_SUM = L_MAX + 64;
constexpr int L_WGT = L_SUM + 64;
constexpr int L_OB = L_WGT + 8;
constexpr int L_T = L_OB + 32;
constexpr int L_TOTAL = L_T + 4;

template <int CTRL> __device__ __forceinline__ float dpp(float x) {
  return __builtin_bit_cast(float, __builtin_amdgcn_mov_dpp(__builtin_bit_cast(int, x), CTRL, 0xf, 0xf, true));
}
constexpr int ROR1 = 0x121, ROR2 = 0x122, ROR4 = 0x124, ROR8 = 0x128;
__device__ __forceinline__ float x16_sum(float x) {
  auto s = __builtin_amdgcn_permlane16_swap(__float_as_uint(x), __float_as_uint(x), false, false);
  return __uint_as_float(s[0]) + __uint_as_float(s[1]);
}
__device__ __forceinline__ float x16_max(float x) {
  auto s = __builtin_amdgcn_permlane16_swap(__float_as_uint(x), __float_as_uint(x), false, false);
  return fmaxf(__uint_as_float(s[0]), __uint_as_float(s[1]));
}
__device__ __forceinline__ float x32_sum(float x) {
  auto t = __builtin_amdgcn_permlane32_swap(__float_as_uint(x), __float_as_uint(x), false, false);
  return __uint_as_float(t[0]) + __uint_as_float(t[1]);
}
__device__ __forceinline__ float x32_max(float x) {
  auto t = __builtin_amdgcn_permlane32_swap(__float_as_uint(x), __float_as_uint(x), false, false);
  return fmaxf(__uint_as_float(t[0]), __uint_as_float(t[1]));
}
__device__ __forceinline__ float dot2(unsigned a, unsigned b, float c) {
  return __builtin_amdgcn_fdot2_f32_bf16(__builtin_bit_cast(bf16x2, a), __builtin_bit_cast(bf16x2, b), c, false);
}
__device__ __forceinline__ unsigned cvtpk(float lo, float hi) {
  unsigned r; asm("v_cvt_pk_bf16_f32 %0, %1, %2" : "=v"(r) : "v"(lo), "v"(hi)); return r;
}
__device__ __forceinline__ f32x4 mfma(v4u a, v4u b, f32x4 c) {
  return __builtin_amdgcn_mfma_f32_16x16x32_bf16(__builtin_bit_cast(bf16x8, a), __builtin_bit_cast(bf16x8, b), c, 0, 0, 0);
}
__device__ __forceinline__ v4u tag4(f32x4 a, v4u t) {
  return v4u{((__float_as_uint(a[0]) + 0x800u) & 0xFFFFF000u) | t.x, ((__float_as_uint(a[1]) + 0x800u) & 0xFFFFF000u) | t.y,
             ((__float_as_uint(a[2]) + 0x800u) & 0xFFFFF000u) | t.z, ((__float_as_uint(a[3]) + 0x800u) & 0xFFFFF000u) | t.w};
}
__device__ __forceinline__ bool bad4(v4u v, v4u t) { return (((v.x ^ t.x) | (v.y ^ t.y) | (v.z ^ t.z) | (v.w ^ t.w)) & 0xFFFu) != 0u; }
__device__ __forceinline__ float val(unsigned u) { return __uint_as_float(u & 0xFFFFF000u); }
__device__ __forceinline__ v2u tr_read(const float* p) {
  return __builtin_bit_cast(v2u, __builtin_amdgcn_ds_read_tr16_b64_v4i16((__attribute__((address_space(3))) v4s*)p));
}
#define SB() __builtin_amdgcn_sched_barrier(0)
#define LDS_BARRIER() do { asm volatile("s_waitcnt lgkmcnt(0)" ::: "memory"); __builtin_amdgcn_s_barrier(); asm volatile("" ::: "memory"); } while (0)
#define STR2(x) #x
#define STR(x) STR2(x)
#define VMWAIT(n) asm volatile("s_waitcnt vmcnt(" STR(n) ")" ::: "memory")
#define STEP_BAR 1
#define FL_SLEEP 4
#define D_PO 70
#define D_F 210
#define STEP() do { if (wv == 7) VMWAIT(8); __builtin_amdgcn_s_barrier(); SB(); } while (0)
#define STEP_LDS() do { if (wv == 7) VMWAIT(8); LDS_BARRIER(); SB(); } while (0)
typedef __attribute__((address_space(3))) unsigned lds_u32;
__device__ __forceinline__ void lds_word_store(float* p, unsigned v) { __hip_atomic_store((lds_u32*)p, v, __ATOMIC_RELAXED, __HIP_MEMORY_SCOPE_WORKGROUP); }
__device__ __forceinline__ unsigned lds_word_load(float* p) { return __hip_atomic_load((lds_u32*)p, __ATOMIC_RELAXED, __HIP_MEMORY_SCOPE_WORKGROUP); }
__device__ __forceinline__ unsigned now32() { return (unsigned)__builtin_amdgcn_s_memrealtime(); }
__device__ __forceinline__ int wave_max_i32(int x) {
  x = max(x, __builtin_amdgcn_mov_dpp(x, 0x121, 0xf, 0xf, true)); x = max(x, __builtin_amdgcn_mov_dpp(x, 0x122, 0xf, 0xf, true));
  x = max(x, __builtin_amdgcn_mov_dpp(x, 0x124, 0xf, 0xf, true)); x = max(x, __builtin_amdgcn_mov_dpp(x, 0x128, 0xf, 0xf, true));
  auto s = __builtin_amdgcn_permlane16_swap((unsigned)x, (unsigned)x, false, false); x = max((int)s[0], (int)s[1]);
  auto t = __builtin_amdgcn_permlane32_swap((unsigned)x, (unsigned)x, false, false); x = max((int)t[0], (int)t[1]);
  return __builtin_amdgcn_readfirstlane(x);
}
__device__ __forceinline__ void wait_until(unsigned ref, int dt) {
  dt = ABL ? 0 : min(dt, 500);
  while ((int)(now32() - ref) < dt) {}
}
#define STAMP(i) do { } while (0)
#define COUNT(i) do { } while (0)

__global__ void __launch_bounds__(NT, 2)
gqa_decode_fused(const u16* __restrict__ x, const u16* __restrict__ kc, const u16* __restrict__ vc,
                 const u16* __restrict__ wq, const u16* __restrict__ wo, u16* __restrict__ out, unsigned char* ws) {
  __shared__ __attribute__((aligned(16))) float lds[L_TOTAL];
  const int tid = threadIdx.x, lane = tid & 63, wv = __builtin_amdgcn_readfirstlane(tid >> 6);
  const int wg = blockIdx.x, xcd = wg & 7, G = xcd >> 1, b = xcd & 1, sp = wg >> 3, idx = b * 32 + sp;
  const int cb = idx >> 2, kq = idx & 3;
  const int hB = idx >> 3, dq = (idx >> 1) & 3, jh = idx & 1;
  const int g = lane >> 4, c = lane & 15;
  STAMP(0);

  constexpr unsigned US = KMAP == 0 ? (2u << 20) : KMAP == 1 ? 65536u : 8192u;
  const unsigned wbase = KMAP == 0 ? (sp * 8 + wv) * 8192u : KMAP == 1 ? sp * 262144u + wv * 8192u : sp * 262144u + wv * 32768u;
  const size_t kvoff = (size_t)(b * 4 + G) * SK * DH * 2 + wbase;
  const __amdgpu_buffer_rsrc_t kR = __builtin_amdgcn_make_buffer_rsrc((void*)((const unsigned char*)kc + kvoff), 0, 3 * US + 8192, 0x00020000);
  const __amdgpu_buffer_rsrc_t vR = __builtin_amdgcn_make_buffer_rsrc((void*)((const unsigned char*)vc + kvoff), 0, 3 * US + 8192, 0x00020000);
  const __amdgpu_buffer_rsrc_t wsR = __builtin_amdgcn_make_buffer_rsrc((void*)ws, 0, (ABL & 1) ? 0u : WS_END, 0x00020000);
#define WSX(bit) __builtin_amdgcn_make_buffer_rsrc((void*)ws, 0, (ABL & (1 | (bit))) ? 0u : WS_END, 0x00020000)

  v4u K[4][2][4];
  v4u V[4][8];
  const int voK = (4 * (c & 3) + ((c >> 2) & 1)) * 256 + (c >> 3) * 64 + g * 16;
  const int voV = lane * 16;
#define LOADK(T, t) do { _Pragma("unroll") for (int p_ = 0; p_ < 4; ++p_) K[T][t][p_] = __builtin_amdgcn_raw_buffer_load_b128(kR, voK + (p_ & 1) * 128 + (p_ >> 1) * 512, (T) * US + (t) * 4096, AUX_NT); SB(); } while (0)
#define LOADV(T, t) do { _Pragma("unroll") for (int j_ = 0; j_ < 4; ++j_) V[T][(t) * 4 + j_] = __builtin_amdgcn_raw_buffer_load_b128(vR, voV + j_ * 1024, (T) * US + (t) * 4096, AUX_NT); SB(); } while (0)

  v4u wqv[4];
  v2u xv[2];
  {
    const __amdgpu_buffer_rsrc_t wqR = __builtin_amdgcn_make_buffer_rsrc((void*)(wq + (size_t)(kq * 256 + wv * 32) * 4096 + G * 1024 + cb * 64), 0, (ABL & 4) ? 0 : 31 * 8192 + 128, 0x00020000);
    const __amdgpu_buffer_rsrc_t xR = __builtin_amdgcn_make_buffer_rsrc((void*)x, 0, 4096, 0x00020000);
#pragma unroll
    for (int i = 0; i < 4; ++i) wqv[i] = __builtin_amdgcn_raw_buffer_load_b128(wqR, (lane >> 3) * 32768 + (lane & 7) * 16, i * 8192, AUX_NT);
    xv[0] = __builtin_amdgcn_raw_buffer_load_b64(xR, (kq * 256 + wv * 32 + 4 * (lane >> 3)) * 2, 0, AUX_NT);
    xv[1] = __builtin_amdgcn_raw_buffer_load_b64(xR, 2048 + (kq * 256 + wv * 32 + 4 * (lane >> 3)) * 2, 0, AUX_NT);
  }
  const v2u ep = __builtin_amdgcn_raw_buffer_load_b64(WSX(2048), WS_EPOCH, 0, AUX_SC1);
  SB();
  __builtin_amdgcn_s_barrier();
  SB();
  LOADK(0, 0); LOADK(0, 1);
  STAMP(14);
  const unsigned off_po = WS_PO + ((G * 2 + b) * 32 + sp) * 4096 + (tid & 255) * 16;
  const unsigned off_f = WS_F + ((jh * 32 + wv * 4 + ((c >> 1) & 3)) * 128 + (G * 8 + hB) * 4 + dq) * 128 + (c & 1) * 64 + g * 16;
  const unsigned off_fl = WS_FL + (G * 64 + idx) * 128;
  {
    STAMP(16);
    float qa[2][8];
#pragma unroll
    for (int e = 0; e < 8; ++e) {
      const unsigned sel = (e & 1) ? 0x07060302u : 0x05040100u;
      const unsigned p01 = __builtin_amdgcn_perm(wqv[1][e >> 1], wqv[0][e >> 1], sel);
      const unsigned p23 = __builtin_amdgcn_perm(wqv[3][e >> 1], wqv[2][e >> 1], sel);
#pragma unroll
      for (int bb = 0; bb < 2; ++bb) {
        float t = dot2(p23, xv[bb].y, dot2(p01, xv[bb].x, 0.f));
        t += dpp<ROR8>(t);
        qa[bb][e] = x32_sum(x16_sum(t));
      }
    }
    if (lane < 8) {
      float* dst = lds + L_QR + wv * 128 + lane * 8;
#pragma unroll
      for (int bb = 0; bb < 2; ++bb) {
        *(f32x4*)(dst + bb * 64) = f32x4{qa[bb][0], qa[bb][1], qa[bb][2], qa[bb][3]};
        *(f32x4*)(dst + bb * 64 + 4) = f32x4{qa[bb][4], qa[bb][5], qa[bb][6], qa[bb][7]};
      }
    }
    SB();
  }
  const unsigned long long tag = (((unsigned long long)__builtin_amdgcn_readfirstlane(ep.y) << 32) | __builtin_amdgcn_readfirstlane(ep.x)) + 1ull;
  const unsigned t40_lo = (unsigned)tag & 0xFFu, t40_hi = (unsigned)(tag >> 8);
  const unsigned t48_lo = (unsigned)tag & 0xFFFFu, t48_hi = (unsigned)(tag >> 16);
  const unsigned long long tw = (tag & 0xFFFFFFFFFFFFull) * 0x001001001001ull;
  const v4u t12 = {(unsigned)tag & 0xFFFu, (unsigned)(tw >> 12) & 0xFFFu, (unsigned)(tw >> 24) & 0xFFFu, (unsigned)(tw >> 36) & 0xFFFu};
  STAMP(17);
  if (tid == 0) lds_word_store(lds + L_T, 0u);
  LDS_BARRIER();
  if (tid < 128) {
    float s0 = 0.f;
#pragma unroll
    for (int w = 0; w < 8; ++w) s0 += lds[L_QR + w * 128 + tid];
    const unsigned off = WS_QP + (((G * 16 + cb) * 4 + ((tid >> 4) & 3)) * 4 + kq) * 256 + (tid >> 6) * 128 + (tid & 15) * 8;
    __builtin_amdgcn_raw_buffer_store_b64(v2u{(__float_as_uint(s0) & 0xFFFFFF00u) | t40_lo, t40_hi}, WSX(16), off, 0, AUX_SC1);
  }
  SB();
  STAMP(1);
  LOADK(1, 0);
  STEP();
  LOADK(1, 1);
  STEP();
  STAMP(15);
  const unsigned off_qp = WS_QP + ((G * 16 + cb) * 4 + kq) * 1024 + lane * 16;
  v4u qp = {0u, 0u, 0u, 0u};
  if (wv == 0) qp = __builtin_amdgcn_raw_buffer_load_b128(WSX(32), off_qp, 0, AUX_SC1);
  SB();
  LOADK(2, 0);
  STEP();
  STAMP(18);
  LOADK(2, 1);
  STEP();
  STAMP(19);
  LOADK(3, 0);
  if (wv == 0) {
#define QP_BAD(v) ((((v).x ^ t40_lo) & 0xFFu) | ((v).y ^ t40_hi) | (((v).z ^ t40_lo) & 0xFFu) | ((v).w ^ t40_hi))
    if (!ABL && !__all(QP_BAD(qp) == 0u)) {
      unsigned spins = 0;
      do {
        __builtin_amdgcn_s_sleep(4); asm volatile("" ::: "memory");
        qp = __builtin_amdgcn_raw_buffer_load_b128(wsR, off_qp, 0, AUX_SC1);
        COUNT(0);
      } while (!__all(QP_BAD(qp) == 0u) && ++spins < SPIN_CAP);
    }
    const float q0 = x32_sum(x16_sum(__uint_as_float(qp.x & 0xFFFFFF00u))), q1 = x32_sum(x16_sum(__uint_as_float(qp.z & 0xFFFFFF00u)));
    const unsigned pk = cvtpk(q0, q1);
    if (lane < 16)
      __builtin_amdgcn_raw_buffer_store_b128(v4u{(pk & 0xFFFFu) | (t48_lo << 16), t48_hi, (pk >> 16) | (t48_lo << 16), t48_hi}, WSX(64),
                                             WS_Q + ((lane >> 3) * 4096 + G * 1024 + idx * 16 + (lane & 7) * 2) * 8, 0, AUX_SC1);
    SB();
  }
  STAMP(2);
  STEP();
  LOADK(3, 1);
  STEP();
  LOADV(0, 0);
  STEP();
  const unsigned off_q = WS_Q + (b * 4096 + G * 1024 + ((wv - 1) & 3) * 256) * 8 + lane * 16;
  v4u qg[2] = {v4u{0u, 0u, 0u, 0u}, v4u{0u, 0u, 0u, 0u}};
  if (wv >= 1 && wv <= 4) {
    qg[0] = __builtin_amdgcn_raw_buffer_load_b128(WSX(128), off_q, 0, AUX_SC1);
    qg[1] = __builtin_amdgcn_raw_buffer_load_b128(WSX(128), off_q + 1024, 0, AUX_SC1);
  }
  SB();
  LOADV(0, 1);
  STEP();
  LOADV(1, 0);
  STEP();
  LOADV(1, 1);
#define Q_BAD(v) ((((v).x >> 16) ^ t48_lo) | ((v).y ^ t48_hi) | (((v).z >> 16) ^ t48_lo) | ((v).w ^ t48_hi))
  if (wv >= 1 && wv <= 4) {
    if (!ABL && !__all((Q_BAD(qg[0]) | Q_BAD(qg[1])) == 0u)) {
      unsigned spins = 0;
      do {
        __builtin_amdgcn_s_sleep(4); asm volatile("" ::: "memory");
        qg[0] = __builtin_amdgcn_raw_buffer_load_b128(wsR, off_q, 0, AUX_SC1);
        qg[1] = __builtin_amdgcn_raw_buffer_load_b128(wsR, off_q + 1024, 0, AUX_SC1);
        COUNT(1);
      } while (!__all((Q_BAD(qg[0]) | Q_BAD(qg[1])) == 0u) && ++spins < SPIN_CAP);
    }
    ((unsigned*)lds)[L_Q + (wv - 1) * 128 + lane] = (qg[0].x & 0xFFFFu) | (qg[0].z << 16);
    ((unsigned*)lds)[L_Q + (wv - 1) * 128 + 64 + lane] = (qg[1].x & 0xFFFFu) | (qg[1].z << 16);
  }
  LDS_BARRIER();
  STAMP(3);
  v4u Q[2];
#pragma unroll
  for (int h = 0; h < 2; ++h) Q[h] = *(const v4u*)(lds + L_Q + (c & 7) * 64 + h * 32 + (c >> 3) * 16 + g * 4);
  f32x4 S[4][2];
#define QK(T) do { _Pragma("unroll") for (int t_ = 0; t_ < 2; ++t_) { const f32x4 z_ = {0.f, 0.f, 0.f, 0.f}; \
    const f32x4 dx_ = mfma(K[T][t_][1], Q[1], mfma(K[T][t_][0], Q[0], z_)), dy_ = mfma(K[T][t_][3], Q[1], mfma(K[T][t_][2], Q[0], z_)); \
    _Pragma("unroll") for (int i_ = 0; i_ < 4; ++i_) { \
      auto sw_ = __builtin_amdgcn_permlane32_swap(__float_as_uint(dx_[i_]), __float_as_uint(dy_[i_]), false, false); \
      S[T][t_][i_] = __uint_as_float(sw_[0]) + dpp<ROR8>(__uint_as_float(sw_[1])); } } SB(); } while (0)
  QK(0); QK(1); QK(2); QK(3);
  constexpr float C2 = 0.08838834764831845f * 1.4426950408889634f;
  float mx = -INFINITY;
#pragma unroll
  for (int T = 0; T < 4; ++T)
#pragma unroll
    for (int t = 0; t < 2; ++t) mx = fmaxf(mx, fmaxf(fmaxf(S[T][t][0], S[T][t][1]), fmaxf(S[T][t][2], S[T][t][3])));
  mx = x32_max(x16_max(mx)) * C2;
  if (lane < 8) lds[L_MAX + lane * 8 + wv] = mx;
  STEP_LDS();
  LOADV(2, 0);
  float M;
  {
    const f32x4 m0 = *(const f32x4*)(lds + L_MAX + (c & 7) * 8), m1 = *(const f32x4*)(lds + L_MAX + (c & 7) * 8 + 4);
    M = fmaxf(fmaxf(fmaxf(m0[0], m0[1]), fmaxf(m0[2], m0[3])), fmaxf(fmaxf(m1[0], m1[1]), fmaxf(m1[2], m1[3])));
    if (c >= 8) M = mx;
  }
  v4u P[4];
  {
    const float msub = (M == -INFINITY) ? 0.f : M;
    float lsum = 0.f;
#pragma unroll
    for (int T = 0; T < 4; ++T) {
      float p[8];
#pragma unroll
      for (int j = 0; j < 8; ++j) { p[j] = __builtin_amdgcn_exp2f(fmaf(S[T][j >> 2][j & 3], C2, -msub)); lsum += p[j]; }
      P[T] = v4u{cvtpk(p[0], p[1]), cvtpk(p[2], p[3]), cvtpk(p[4], p[5]), cvtpk(p[6], p[7])};
    }
    lsum = x32_sum(x16_sum(lsum));
    if (lane < 8) lds[L_SUM + lane * 8 + wv] = lsum;
  }
  LDS_BARRIER();
  if (tid < 16) {
    const f32x4 l0 = *(const f32x4*)(lds + L_SUM + (lane & 7) * 8), l1 = *(const f32x4*)(lds + L_SUM + (lane & 7) * 8 + 4);
    const float L = ((l0[0] + l0[1]) + (l0[2] + l0[3])) + ((l1[0] + l1[1]) + (l1[2] + l1[3]));
    const unsigned u = __float_as_uint(lane < 8 ? M : L);
    __builtin_amdgcn_raw_buffer_store_b128(v4u{(u & 0xFFFFu) | (t48_lo << 16), t48_hi, (u >> 16) | (t48_lo << 16), t48_hi}, WSX(512),
                                           WS_ML + ((b * 4 + G) * 32 + sp) * 256 + (lane & 7) * 32 + (lane >> 3) * 16, 0, AUX_SC1);
  }
  SB();
  STAMP(4);
  STEP();
  LOADV(2, 1);
  f32x4 O[8];
#pragma unroll
  for (int e = 0; e < 8; ++e) O[e] = f32x4{0.f, 0.f, 0.f, 0.f};
#define PV(T) do { _Pragma("unroll") for (int e_ = 0; e_ < 8; ++e_) { \
      const unsigned sel_ = (e_ & 1) ? 0x07060302u : 0x05040100u; v4u a_; \
      _Pragma("unroll") for (int d_ = 0; d_ < 4; ++d_) a_[d_] = __builtin_amdgcn_perm(V[T][2 * d_ + 1][e_ >> 1], V[T][2 * d_][e_ >> 1], sel_); \
      O[e_] = mfma(a_, P[T], O[e_]); } SB(); } while (0)
  PV(0);
  STEP();
  LOADV(3, 0);
  PV(1);
  STEP();
  LOADV(3, 1);
  STEP();
  const __amdgpu_buffer_rsrc_t wsA = __builtin_amdgcn_make_buffer_rsrc((void*)ws, 0, (wv < 4 && !(ABL & (1 | 1024))) ? WS_END : 0u, 0x00020000);
  const unsigned off_mg = WS_ML + ((b * 4 + G) * 32 + (lane >> 1)) * 256 + (wv & 3) * 64 + (lane & 1) * 16;
  v4u ml[2];
  ml[0] = __builtin_amdgcn_raw_buffer_load_b128(wsA, off_mg, 0, AUX_SC1);
  ml[1] = __builtin_amdgcn_raw_buffer_load_b128(wsA, off_mg + 32, 0, AUX_SC1);
  v4u wov[4];
  {
    const __amdgpu_buffer_rsrc_t woR = __builtin_amdgcn_make_buffer_rsrc((void*)(wo + (size_t)((G * 8 + hB) * 128 + dq * 32) * 1024 + jh * 512 + wv * 64), 0, (ABL & 8) ? 0 : 31 * 2048 + 128, 0x00020000);
#pragma unroll
    for (int i = 0; i < 4; ++i) wov[i] = __builtin_amdgcn_raw_buffer_load_b128(woR, (lane >> 3) * 2048 + (lane & 7) * 16, i * 16384, AUX_NT);
    SB();
  }
  PV(2);
  STAMP(5);
  VMWAIT(6);
  SB();
  if (tid == 448) __builtin_amdgcn_raw_buffer_store_b64(v2u{now32(), (unsigned)tag}, wsR, off_fl, 0, AUX_SC1);
  const unsigned off_fg = WS_FL + (G * 64 + lane) * 128;
  v2u fl = __builtin_amdgcn_raw_buffer_load_b64(__builtin_amdgcn_make_buffer_rsrc((void*)ws, 0, (wv == 0 && !(ABL & 1)) ? WS_END : 0u, 0x00020000), off_fg, 0, AUX_SC1);
  SB();
  PV(3);
  STAMP(6);
  if (c < 8) {
    float* dst = lds + L_RED + (wv * 8 + c) * RED_STRIDE + 32 * g;
#pragma unroll
    for (int i = 0; i < 4; ++i) {
      *(f32x4*)(dst + 8 * i) = f32x4{O[0][i], O[1][i], O[2][i], O[3][i]};
      *(f32x4*)(dst + 8 * i + 4) = f32x4{O[4][i], O[5][i], O[6][i], O[7][i]};
    }
  }
  if (wv < 4) {
    if (!ABL && !__all((Q_BAD(ml[0]) | Q_BAD(ml[1])) == 0u)) {
      unsigned spins = 0;
      do {
        __builtin_amdgcn_s_sleep(2); asm volatile("" ::: "memory");
        ml[0] = __builtin_amdgcn_raw_buffer_load_b128(wsR, off_mg, 0, AUX_SC1);
        ml[1] = __builtin_amdgcn_raw_buffer_load_b128(wsR, off_mg + 32, 0, AUX_SC1);
        COUNT(2);
      } while (!__all((Q_BAD(ml[0]) | Q_BAD(ml[1])) == 0u) && ++spins < SPIN_CAP);
    }
#pragma unroll
    for (int k = 0; k < 2; ++k) {
      const float v = __uint_as_float((ml[k].x & 0xFFFFu) | (ml[k].z << 16)), vo = dpp<0xB1>(v);
      const float ms = (lane & 1) ? vo : v, ls = (lane & 1) ? v : vo;
      float Mg = fmaxf(ms, dpp<ROR2>(ms)); Mg = fmaxf(Mg, dpp<ROR4>(Mg)); Mg = fmaxf(Mg, dpp<ROR8>(Mg)); Mg = x32_max(x16_max(Mg));
      const float wgt = (ls > 0.f) ? __builtin_amdgcn_exp2f(ms - Mg) : 0.f;
      float Lt = wgt * ls; Lt += dpp<ROR2>(Lt); Lt += dpp<ROR4>(Lt); Lt += dpp<ROR8>(Lt); Lt = x32_sum(x16_sum(Lt));
      if (lane == 2 * sp) lds[L_WGT + wv * 2 + k] = wgt / Lt;
    }
  }
  LDS_BARRIER();
  STAMP(7);
  if (wv >= 4) {
    const int h = (tid >> 5) & 7, d4 = tid & 31;
    f32x4 a = *(const f32x4*)(lds + L_RED + h * RED_STRIDE + 4 * d4);
#pragma unroll
    for (int w = 1; w < 8; ++w) a += *(const f32x4*)(lds + L_RED + (w * 8 + h) * RED_STRIDE + 4 * d4);
    a *= lds[L_WGT + h];
    __builtin_amdgcn_raw_buffer_store_b128(tag4(a, t12), wsR, off_po, 0, AUX_SC1);
  }
  STAMP(8);
  v4u WA[4];
  {
    float* wt = lds + L_WOT + wv * 32 * WOT_STRIDE;
#pragma unroll
    for (int i = 0; i < 4; ++i) *(v4u*)(wt + (8 * i + (lane >> 3)) * WOT_STRIDE + (lane & 7) * 4) = wov[i];
#pragma unroll
    for (int u = 0; u < 4; ++u) {
      const float* p0 = wt + (8 * g + (c >> 2)) * WOT_STRIDE + u * 8 + (c & 3) * 2;
      const v2u lo = tr_read(p0), hi = tr_read(p0 + 4 * WOT_STRIDE);
      WA[u] = v4u{lo.x, lo.y, hi.x, hi.y};
    }
    SB();
  }
  v2u fa[4];
  if (wv < 4) {
    unsigned spins = 0;
    if (wv == 0) {
      while (__any(fl.y != (unsigned)tag) && ++spins < SPIN_CAP) {
        __builtin_amdgcn_s_sleep(FL_SLEEP); asm volatile("" ::: "memory");
        if (fl.y != (unsigned)tag) fl = __builtin_amdgcn_raw_buffer_load_b64(wsR, off_fg, 0, AUX_SC1);
      }
      const unsigned ref = now32();
      const unsigned t = (ref + (unsigned)min(wave_max_i32((int)(fl.x - ref)) + D_PO, 500)) | 1u;
      if (lane == 0) lds_word_store(lds + L_T, t);
      wait_until(ref, (int)(t - ref));
    } else {
      unsigned t;
      while ((t = lds_word_load(lds + L_T)) == 0u) __builtin_amdgcn_s_sleep(1);
      const unsigned ref = now32();
      wait_until(ref, (int)(t - ref));
    }
    const unsigned off_pg = WS_PO + ((G * 2 + (wv >> 1)) * 32 + (lane >> 2)) * 4096 + hB * 512 + dq * 128 + (wv & 1) * 64 + (lane & 3) * 16;
    v4u pa[2];
    pa[0] = __builtin_amdgcn_raw_buffer_load_b128(wsR, off_pg, 0, AUX_SC1);
    pa[1] = __builtin_amdgcn_raw_buffer_load_b128(wsR, off_pg + 65536, 0, AUX_SC1);
    if (wv == 0) {
#pragma unroll
      for (int k = 0; k < 4; ++k) fa[k] = __builtin_amdgcn_raw_buffer_load_b64(wsR, WS_FL + lane * 128, k * 8192, AUX_SC1);
    }
    spins = 0;
    for (;;) {
      const bool bad0 = bad4(pa[0], t12), bad1 = bad4(pa[1], t12);
      if (!__any(bad0 | bad1) || ++spins > SPIN_CAP) break;
      asm volatile("" ::: "memory");
      if (bad0) pa[0] = __builtin_amdgcn_raw_buffer_load_b128(wsR, off_pg, 0, AUX_SC1);
      if (bad1) pa[1] = __builtin_amdgcn_raw_buffer_load_b128(wsR, off_pg + 65536, 0, AUX_SC1);
      COUNT(3);
    }
    STAMP(10);
    float o[4];
#pragma unroll
    for (int i = 0; i < 4; ++i) {
      float t = val(pa[0][i]) + val(pa[1][i]);
      t += dpp<ROR4>(t); t += dpp<ROR8>(t);
      o[i] = x32_sum(x16_sum(t));
    }
    if (lane < 4) *(v2u*)(lds + L_OB + (wv >> 1) * 16 + (wv & 1) * 8 + lane * 2) = v2u{cvtpk(o[0], o[1]), cvtpk(o[2], o[3])};
  }
#define FA_BAD (((fa[0].y ^ (unsigned)tag) | (fa[1].y ^ (unsigned)tag) | (fa[2].y ^ (unsigned)tag) | (fa[3].y ^ (unsigned)tag)) != 0u)
  int fa_max = 0; bool fa_all = false; unsigned fa_ref = 0;
  if (wv == 0) {
    fa_all = !__any(FA_BAD);
    fa_ref = now32();
    fa_max = wave_max_i32(max(max((int)(fa[0].x - fa_ref), (int)(fa[1].x - fa_ref)), max((int)(fa[2].x - fa_ref), (int)(fa[3].x - fa_ref))));
  }
  LDS_BARRIER();
  STAMP(11);
  {
    const v4u ob = *(const v4u*)(lds + L_OB + (c & 1) * 16 + g * 4);
    f32x4 d = {0.f, 0.f, 0.f, 0.f};
#pragma unroll
    for (int u = 0; u < 4; ++u) {
      const bool on = (c >> 1) == u;
      d = mfma(WA[u], v4u{on ? ob.x : 0u, on ? ob.y : 0u, on ? ob.z : 0u, on ? ob.w : 0u}, d);
    }
    if (c < 8) __builtin_amdgcn_raw_buffer_store_b128(tag4(d, t12), wsR, off_f, 0, AUX_SC1);
  }
  STAMP(12);
  if (wv == 0) {
    unsigned spins = 0;
    if (!fa_all) {
      do {
        __builtin_amdgcn_s_sleep(FL_SLEEP); asm volatile("" ::: "memory");
#pragma unroll
        for (int k = 0; k < 4; ++k) if (fa[k].y != (unsigned)tag) fa[k] = __builtin_amdgcn_raw_buffer_load_b64(wsR, WS_FL + lane * 128, k * 8192, AUX_SC1);
      } while (__any(FA_BAD) && ++spins < SPIN_CAP);
      fa_ref = now32();
      fa_max = wave_max_i32(max(max((int)(fa[0].x - fa_ref), (int)(fa[1].x - fa_ref)), max((int)(fa[2].x - fa_ref), (int)(fa[3].x - fa_ref))));
    }
    wait_until(fa_ref, fa_max + D_F);
    const int cq = xcd * 32 + sp;
    const unsigned off = WS_F + (cq >> 2) * 16384 + (lane >> 1) * 128 + (lane & 1) * 64 + (cq & 3) * 16;
    v4u f[4];
#pragma unroll
    for (int k = 0; k < 4; ++k) f[k] = __builtin_amdgcn_raw_buffer_load_b128(wsR, off + k * 4096, 0, AUX_SC1);
    spins = 0;
    for (;;) {
      bool bad[4];
#pragma unroll
      for (int k = 0; k < 4; ++k) bad[k] = bad4(f[k], t12);
      if (!__any(bad[0] | bad[1] | bad[2] | bad[3]) || ++spins > SPIN_CAP) break;
      asm volatile("" ::: "memory");
#pragma unroll
      for (int k = 0; k < 4; ++k) if (bad[k]) f[k] = __builtin_amdgcn_raw_buffer_load_b128(wsR, off + k * 4096, 0, AUX_SC1);
      COUNT(4);
    }
    STAMP(13);
    float s[4];
#pragma unroll
    for (int i = 0; i < 4; ++i) {
      float t = (val(f[0][i]) + val(f[1][i])) + (val(f[2][i]) + val(f[3][i]));
      t += dpp<ROR2>(t); t += dpp<ROR4>(t); t += dpp<ROR8>(t);
      s[i] = x32_sum(x16_sum(t));
    }
    if (lane < 2) *(v2u*)(out + lane * 1024 + cq * 4) = v2u{cvtpk(s[0], s[1]), cvtpk(s[2], s[3])};
    if (wg == 0 && lane == 0) __builtin_amdgcn_raw_buffer_store_b64(v2u{(unsigned)tag, (unsigned)(tag >> 32)}, wsR, WS_EPOCH, 0, AUX_SC1);
  }
  STAMP(9);
}

extern "C" void kernel_launch(void* const* d_in, const int* in_sizes, int n_in,
                              void* d_out, int out_size, void* d_ws, size_t ws_size,
                              hipStream_t stream) {
  (void)in_sizes; (void)n_in; (void)out_size; (void)ws_size;
  gqa_decode_fused<<<NWG, NT, 0, stream>>>((const u16*)d_in[0], (const u16*)d_in[1], (const u16*)d_in[2],
                                           (const u16*)d_in[3], (const u16*)d_in[4], (u16*)d_out, (unsigned char*)d_ws);
}
```

```cpp
#include <hip/hip_runtime.h>
#include <hip/hip_bf16.h>
#include <stdint.h>

typedef unsigned v4u __attribute__((ext_vector_type(4)));
typedef unsigned v3u __attribute__((ext_vector_type(3)));
typedef unsigned v2u __attribute__((ext_vector_type(2)));
typedef float f32x4 __attribute__((ext_vector_type(4)));
typedef float f32x2 __attribute__((ext_vector_type(2)));
typedef short bf16x8 __attribute__((ext_vector_type(8)));
typedef short v4s __attribute__((ext_vector_type(4)));
typedef __bf16 bf16x2 __attribute__((ext_vector_type(2)));
typedef unsigned short u16;
typedef __attribute__((address_space(3))) unsigned lds_u32;

constexpr int AUX_NT = 2, AUX_SC1 = 16;
constexpr int SK = 32768, DH = 128;
constexpr int NWG = 256, NT = 512;
constexpr unsigned SPIN_CAP = 1u << 16;
constexpr int D_PO = 95, D_PO1 = 100, D_F = 285;

constexpr unsigned WS_EPOCH = 0;
constexpr unsigned WS_QP = 4096;
constexpr unsigned WS_Q = WS_QP + 262144;
constexpr unsigned WS_ML = WS_Q + 65536;
constexpr unsigned WS_PO = WS_ML + 65536;
constexpr unsigned WS_F = WS_PO + (1u << 20);
constexpr unsigned WS_FL = WS_F + (1u << 20);
constexpr unsigned WS_END = WS_FL + 32768;

constexpr int RED_STRIDE = 132, WOT_STRIDE = 36;
constexpr int L_RED = 0;
constexpr int L_WOT = L_RED + 8 * 8 * RED_STRIDE;
constexpr int L_Q = L_WOT + 8 * 32 * WOT_STRIDE;
constexpr int L_QR = L_Q + 512;
constexpr int L_MAX = L_QR + 1024;
constexpr int L_SUM = L_MAX + 64;
constexpr int L_WGT = L_SUM + 64;
constexpr int L_OB = L_WGT + 8;
constexpr int L_T = L_OB + 32;
constexpr int L_TOTAL = L_T + 4;

template <int CTRL> __device__ __forceinline__ float dpp(float x) {
  return __builtin_bit_cast(float, __builtin_amdgcn_mov_dpp(__builtin_bit_cast(int, x), CTRL, 0xf, 0xf, true));
}
constexpr int ROR2 = 0x122, ROR4 = 0x124, ROR8 = 0x128, XOR1 = 0xB1;
__device__ __forceinline__ float x16_sum(float x) {
  auto s = __builtin_amdgcn_permlane16_swap(__float_as_uint(x), __float_as_uint(x), false, false);
  return __uint_as_float(s[0]) + __uint_as_float(s[1]);
}
__device__ __forceinline__ float x16_max(float x) {
  auto s = __builtin_amdgcn_permlane16_swap(__float_as_uint(x), __float_as_uint(x), false, false);
  return fmaxf(__uint_as_float(s[0]), __uint_as_float(s[1]));
}
__device__ __forceinline__ float x32_sum(float x) {
  auto t = __builtin_amdgcn_permlane32_swap(__float_as_uint(x), __float_as_uint(x), false, false);
  return __uint_as_float(t[0]) + __uint_as_float(t[1]);
}
__device__ __forceinline__ float x32_max(float x) {
  auto t = __builtin_amdgcn_permlane32_swap(__float_as_uint(x), __float_as_uint(x), false, false);
  return fmaxf(__uint_as_float(t[0]), __uint_as_float(t[1]));
}
__device__ __forceinline__ float dot2(unsigned a, unsigned b, float c) {
  return __builtin_amdgcn_fdot2_f32_bf16(__builtin_bit_cast(bf16x2, a), __builtin_bit_cast(bf16x2, b), c, false);
}
__device__ __forceinline__ unsigned cvtpk(float lo, float hi) {
  return __builtin_bit_cast(unsigned, __builtin_convertvector(f32x2{lo, hi}, bf16x2));
}
__device__ __forceinline__ f32x4 mfma16(v2u a, v2u b, f32x4 c) {
  return __builtin_amdgcn_mfma_f32_16x16x16bf16_1k(__builtin_bit_cast(v4s, a), __builtin_bit_cast(v4s, b), c, 0, 0, 0);
}
__device__ __forceinline__ f32x4 mfma(v4u a, v4u b, f32x4 c) {
  return __builtin_amdgcn_mfma_f32_16x16x32_bf16(__builtin_bit_cast(bf16x8, a), __builtin_bit_cast(bf16x8, b), c, 0, 0, 0);
}
__device__ __forceinline__ v4u tag4(f32x4 a, v4u t) {
  return v4u{((__float_as_uint(a[0]) + 0x800u) & 0xFFFFF000u) | t.x, ((__float_as_uint(a[1]) + 0x800u) & 0xFFFFF000u) | t.y,
             ((__float_as_uint(a[2]) + 0x800u) & 0xFFFFF000u) | t.z, ((__float_as_uint(a[3]) + 0x800u) & 0xFFFFF000u) | t.w};
}
__device__ __forceinline__ bool bad4(v4u v, v4u t) { return (((v.x ^ t.x) | (v.y ^ t.y) | (v.z ^ t.z) | (v.w ^ t.w)) & 0xFFFu) != 0u; }
__device__ __forceinline__ float val(unsigned u) { return __uint_as_float(u & 0xFFFFF000u); }
__device__ __forceinline__ v2u tr_read(const float* p) {
  return __builtin_bit_cast(v2u, __builtin_amdgcn_ds_read_tr16_b64_v4i16((__attribute__((address_space(3))) v4s*)p));
}
__device__ __forceinline__ void lds_word_store(float* p, unsigned v) { __hip_atomic_store((lds_u32*)p, v, __ATOMIC_RELAXED, __HIP_MEMORY_SCOPE_WORKGROUP); }
__device__ __forceinline__ unsigned lds_word_load(float* p) { return __hip_atomic_load((lds_u32*)p, __ATOMIC_RELAXED, __HIP_MEMORY_SCOPE_WORKGROUP); }
__device__ __forceinline__ unsigned now32() { return (unsigned)__builtin_amdgcn_s_memrealtime(); }
__device__ __forceinline__ int wave_max_i32(int x) {
  x = max(x, __builtin_amdgcn_mov_dpp(x, 0x121, 0xf, 0xf, true)); x = max(x, __builtin_amdgcn_mov_dpp(x, 0x122, 0xf, 0xf, true));
  x = max(x, __builtin_amdgcn_mov_dpp(x, 0x124, 0xf, 0xf, true)); x = max(x, __builtin_amdgcn_mov_dpp(x, 0x128, 0xf, 0xf, true));
  auto s = __builtin_amdgcn_permlane16_swap((unsigned)x, (unsigned)x, false, false); x = max((int)s[0], (int)s[1]);
  auto t = __builtin_amdgcn_permlane32_swap((unsigned)x, (unsigned)x, false, false); x = max((int)t[0], (int)t[1]);
  return __builtin_amdgcn_readfirstlane(x);
}
__device__ __forceinline__ void wait_until(unsigned ref, int dt) {
  dt = min(dt, 500);
  while ((int)(now32() - ref) < dt) {}
}
#define SB() __builtin_amdgcn_sched_barrier(0)
#define RELOAD() asm volatile("" ::: "memory")
#define LDS_BARRIER() do { asm volatile("s_waitcnt lgkmcnt(0)" ::: "memory"); __builtin_amdgcn_s_barrier(); asm volatile("" ::: "memory"); } while (0)
#define VMWAIT(n) asm volatile("s_waitcnt vmcnt(" #n ")" ::: "memory")
#define STEP() do { if (wv == 7) VMWAIT(8); __builtin_amdgcn_s_barrier(); SB(); } while (0)
#define STEP_LDS() do { if (wv == 7) VMWAIT(8); LDS_BARRIER(); SB(); } while (0)
#define LD128(off) __builtin_amdgcn_raw_buffer_load_b128(wsR, (off), 0, AUX_SC1)

__global__ void __launch_bounds__(NT, 2)
gqa_decode_fused(const u16* __restrict__ x, const u16* __restrict__ kc, const u16* __restrict__ vc,
                 const u16* __restrict__ wq, const u16* __restrict__ wo, u16* __restrict__ out, unsigned char* ws) {
  __shared__ __attribute__((aligned(16))) float lds[L_TOTAL];
  const int tid = threadIdx.x, lane = tid & 63, wv = __builtin_amdgcn_readfirstlane(tid >> 6);
  const int wg = blockIdx.x, xcd = wg & 7, G = xcd >> 1, b = xcd & 1, sp = wg >> 3, idx = b * 32 + sp;
  const int cb = sp >> 1, kq = b * 2 + (sp & 1);
  const int hB = idx >> 3, dq = (idx >> 1) & 3, jh = idx & 1;
  const int g = lane >> 4, c = lane & 15;

  constexpr unsigned US = 2u << 20;
  const size_t kvoff = (size_t)(b * 4 + G) * SK * DH * 2 + (sp * 8 + wv) * 8192u;
  const __amdgpu_buffer_rsrc_t kR = __builtin_amdgcn_make_buffer_rsrc((void*)((const unsigned char*)kc + kvoff), 0, 3 * US + 8192, 0x00020000);
  const __amdgpu_buffer_rsrc_t vR = __builtin_amdgcn_make_buffer_rsrc((void*)((const unsigned char*)vc + kvoff), 0, 3 * US + 8192, 0x00020000);
  const __amdgpu_buffer_rsrc_t wsR = __builtin_amdgcn_make_buffer_rsrc((void*)ws, 0, WS_END, 0x00020000);

  v4u K[4][2][4];
  v4u V[4][8];
  const int voK = (4 * (c & 3) + ((c >> 2) & 1)) * 256 + (c >> 3) * 64 + g * 16;
  const int voV = lane * 16;
#define LOADK(T, t) do { _Pragma("unroll") for (int p_ = 0; p_ < 4; ++p_) K[T][t][p_] = __builtin_amdgcn_raw_buffer_load_b128(kR, voK + (p_ & 1) * 128 + (p_ >> 1) * 512, (T) * US + (t) * 4096, AUX_NT); SB(); } while (0)
#define LOADV(T, t) do { _Pragma("unroll") for (int j_ = 0; j_ < 4; ++j_) V[T][(t) * 4 + j_] = __builtin_amdgcn_raw_buffer_load_b128(vR, voV + j_ * 1024, (T) * US + (t) * 4096, AUX_NT); SB(); } while (0)

  v4u wqv[6];
  v3u xv[2];
  {
    const int nr = b ? 2 : 6, row0 = (b ? 768 + (sp & 1) * 128 : (sp & 1) * 384) + wv * 8 * nr;
    const __amdgpu_buffer_rsrc_t wqR = __builtin_amdgcn_make_buffer_rsrc((void*)(wq + (size_t)row0 * 4096 + G * 1024 + cb * 64), 0, (8 * nr - 1) * 8192 + 128, 0x00020000);
    const __amdgpu_buffer_rsrc_t xR = __builtin_amdgcn_make_buffer_rsrc((void*)x, 0, 4096, 0x00020000);
    const int vo = (lane >> 3) * nr * 8192 + (lane & 7) * 16, xo = (row0 + nr * (lane >> 3)) * 2;
    wqv[0] = __builtin_amdgcn_raw_buffer_load_b128(wqR, vo, 0, AUX_NT);
    wqv[1] = __builtin_amdgcn_raw_buffer_load_b128(wqR, vo, 8192, AUX_NT);
    if (b == 0) {
#pragma unroll
      for (int i = 2; i < 6; ++i) wqv[i] = __builtin_amdgcn_raw_buffer_load_b128(wqR, vo, i * 8192, AUX_NT);
      xv[0] = __builtin_amdgcn_raw_buffer_load_b96(xR, xo, 0, AUX_NT);
      xv[1] = __builtin_amdgcn_raw_buffer_load_b96(xR, 2048 + xo, 0, AUX_NT);
    } else {
#pragma unroll
      for (int i = 2; i < 6; ++i) wqv[i] = v4u{0u, 0u, 0u, 0u};
      xv[0] = v3u{__builtin_amdgcn_raw_buffer_load_b32(xR, xo, 0, AUX_NT), 0u, 0u};
      xv[1] = v3u{__builtin_amdgcn_raw_buffer_load_b32(xR, 2048 + xo, 0, AUX_NT), 0u, 0u};
    }
  }
  const v2u ep = __builtin_amdgcn_raw_buffer_load_b64(wsR, WS_EPOCH, 0, AUX_SC1);
  SB();
  __builtin_amdgcn_s_barrier();
  SB();
  LOADK(0, 0); LOADK(0, 1);
  {
    float qa[2][8];
#pragma unroll
    for (int e = 0; e < 8; ++e) {
      const unsigned sel = (e & 1) ? 0x07060302u : 0x05040100u;
      const unsigned p01 = __builtin_amdgcn_perm(wqv[1][e >> 1], wqv[0][e >> 1], sel);
      const unsigned p23 = __builtin_amdgcn_perm(wqv[3][e >> 1], wqv[2][e >> 1], sel);
      const unsigned p45 = __builtin_amdgcn_perm(wqv[5][e >> 1], wqv[4][e >> 1], sel);
#pragma unroll
      for (int bb = 0; bb < 2; ++bb) {
        float t = dot2(p45, xv[bb].z, dot2(p23, xv[bb].y, dot2(p01, xv[bb].x, 0.f)));
        t += dpp<ROR8>(t);
        qa[bb][e] = x32_sum(x16_sum(t));
      }
    }
    if (lane < 8) {
      float* dst = lds + L_QR + wv * 128 + lane * 8;
#pragma unroll
      for (int bb = 0; bb < 2; ++bb) {
        *(f32x4*)(dst + bb * 64) = f32x4{qa[bb][0], qa[bb][1], qa[bb][2], qa[bb][3]};
        *(f32x4*)(dst + bb * 64 + 4) = f32x4{qa[bb][4], qa[bb][5], qa[bb][6], qa[bb][7]};
      }
    }
    SB();
  }
  const unsigned long long tag = (((unsigned long long)__builtin_amdgcn_readfirstlane(ep.y) << 32) | __builtin_amdgcn_readfirstlane(ep.x)) + 1ull;
  const unsigned t32 = (unsigned)tag;
  const unsigned t40_lo = (unsigned)tag & 0xFFu, t40_hi = (unsigned)(tag >> 8);
  const unsigned t48_lo = (unsigned)tag & 0xFFFFu, t48_hi = (unsigned)(tag >> 16);
  const unsigned long long tw = (tag & 0xFFFFFFFFFFFFull) * 0x001001001001ull;
  const v4u t12 = {(unsigned)tag & 0xFFFu, (unsigned)(tw >> 12) & 0xFFFu, (unsigned)(tw >> 24) & 0xFFFu, (unsigned)(tw >> 36) & 0xFFFu};
  if (tid == 0) { lds_word_store(lds + L_T, 0u); lds_word_store(lds + L_T + 1, 0u); }
  LDS_BARRIER();
  if (tid < 128) {
    float s0 = 0.f;
#pragma unroll
    for (int w = 0; w < 8; ++w) s0 += lds[L_QR + w * 128 + tid];
    const unsigned off = WS_QP + (((G * 16 + cb) * 4 + ((tid >> 4) & 3)) * 4 + kq) * 256 + (tid >> 6) * 128 + (tid & 15) * 8;
    __builtin_amdgcn_raw_buffer_store_b64(v2u{(__float_as_uint(s0) & 0xFFFFFF00u) | t40_lo, t40_hi}, wsR, off, 0, AUX_SC1);
  }
  SB();
  LOADK(1, 0);
  STEP();
  LOADK(1, 1);
  STEP();
  const unsigned off_qp = WS_QP + ((G * 16 + cb) * 4 + kq) * 1024 + lane * 16;
  v4u qp = {0u, 0u, 0u, 0u};
  const bool q_early = wv == 0 && !b, q_late = wv == 0 && b;
  if (q_early) qp = LD128(off_qp);
  SB();
  LOADK(2, 0);
  STEP();
  if (q_late) qp = LD128(off_qp);
  SB();
  LOADK(2, 1);
  STEP();
  LOADK(3, 0);
#define QP_BAD(v) ((((v).x ^ t40_lo) & 0xFFu) | ((v).y ^ t40_hi) | (((v).z ^ t40_lo) & 0xFFu) | ((v).w ^ t40_hi))
#define HOP2() do { \
    if (!__all(QP_BAD(qp) == 0u)) { \
      unsigned spins = 0; \
      do { __builtin_amdgcn_s_sleep(4); RELOAD(); qp = LD128(off_qp); } while (!__all(QP_BAD(qp) == 0u) && ++spins < SPIN_CAP); \
    } \
    const float q0 = x32_sum(x16_sum(__uint_as_float(qp.x & 0xFFFFFF00u))), q1 = x32_sum(x16_sum(__uint_as_float(qp.z & 0xFFFFFF00u))); \
    const unsigned pk = cvtpk(q0, q1);                   \
    if (lane < 16) __builtin_amdgcn_raw_buffer_store_b128(v4u{(pk & 0xFFFFu) | (t48_lo << 16), t48_hi, (pk >> 16) | (t48_lo << 16), t48_hi}, wsR, \
                                             WS_Q + ((lane >> 3) * 4096 + G * 1024 + (cb * 4 + kq) * 16 + (lane & 7) * 2) * 8, 0, AUX_SC1); \
    SB(); } while (0)
  if (q_early) HOP2();
  STEP();
  LOADK(3, 1);
  if (q_late) HOP2();
  STEP();
  LOADV(0, 0);
  STEP();
  const unsigned off_q = WS_Q + (b * 4096 + G * 1024 + ((wv - 1) & 3) * 256) * 8 + lane * 16;
  v4u qg[2] = {v4u{0u, 0u, 0u, 0u}, v4u{0u, 0u, 0u, 0u}};
  if (wv >= 1 && wv <= 4) { qg[0] = LD128(off_q); qg[1] = LD128(off_q + 1024); }
  SB();
  LOADV(0, 1);
  STEP();
  LOADV(1, 0);
  STEP();
  LOADV(1, 1);
#define Q_BAD(v) ((((v).x >> 16) ^ t48_lo) | ((v).y ^ t48_hi) | (((v).z >> 16) ^ t48_lo) | ((v).w ^ t48_hi))
  if (wv >= 1 && wv <= 4) {
    if (!__all((Q_BAD(qg[0]) | Q_BAD(qg[1])) == 0u)) {
      unsigned spins = 0;
      do {
        __builtin_amdgcn_s_sleep(4); RELOAD();
        qg[0] = LD128(off_q); qg[1] = LD128(off_q + 1024);
      } while (!__all((Q_BAD(qg[0]) | Q_BAD(qg[1])) == 0u) && ++spins < SPIN_CAP);
    }
    ((unsigned*)lds)[L_Q + (wv - 1) * 128 + lane] = (qg[0].x & 0xFFFFu) | (qg[0].z << 16);
    ((unsigned*)lds)[L_Q + (wv - 1) * 128 + 64 + lane] = (qg[1].x & 0xFFFFu) | (qg[1].z << 16);
  }
  LDS_BARRIER();
  v4u Q[2];
#pragma unroll
  for (int h = 0; h < 2; ++h) Q[h] = *(const v4u*)(lds + L_Q + (c & 7) * 64 + h * 32 + (c >> 3) * 16 + g * 4);
  f32x4 S[4][2];
#define QK(T) do { _Pragma("unroll") for (int t_ = 0; t_ < 2; ++t_) { const f32x4 z_ = {0.f, 0.f, 0.f, 0.f}; \
    const f32x4 dx_ = mfma(K[T][t_][1], Q[1], mfma(K[T][t_][0], Q[0], z_)), dy_ = mfma(K[T][t_][3], Q[1], mfma(K[T][t_][2], Q[0], z_)); \
    _Pragma("unroll") for (int i_ = 0; i_ < 4; ++i_) { \
      auto sw_ = __builtin_amdgcn_permlane32_swap(__float_as_uint(dx_[i_]), __float_as_uint(dy_[i_]), false, false); \
      S[T][t_][i_] = __uint_as_float(sw_[0]) + dpp<ROR8>(__uint_as_float(sw_[1])); } } SB(); } while (0)
  QK(0); QK(1); QK(2); QK(3);
  constexpr float C2 = 0.08838834764831845f * 1.4426950408889634f;
  float mx = -INFINITY;
#pragma unroll
  for (int T = 0; T < 4; ++T)
#pragma unroll
    for (int t = 0; t < 2; ++t) mx = fmaxf(mx, fmaxf(fmaxf(S[T][t][0], S[T][t][1]), fmaxf(S[T][t][2], S[T][t][3])));
  mx = x32_max(x16_max(mx)) * C2;
  if (lane < 8) lds[L_MAX + lane * 8 + wv] = mx;
  STEP_LDS();
  LOADV(2, 0);
  float M;
  {
    const f32x4 m0 = *(const f32x4*)(lds + L_MAX + (c & 7) * 8), m1 = *(const f32x4*)(lds + L_MAX + (c & 7) * 8 + 4);
    M = fmaxf(fmaxf(fmaxf(m0[0], m0[1]), fmaxf(m0[2], m0[3])), fmaxf(fmaxf(m1[0], m1[1]), fmaxf(m1[2], m1[3])));
    if (c >= 8) M = mx;
  }
  v4u P[4];
  {
    const float msub = (M == -INFINITY) ? 0.f : M;
    float lsum = 0.f;
#pragma unroll
    for (int T = 0; T < 4; ++T) {
      float p[8];
#pragma unroll
      for (int j = 0; j < 8; ++j) { p[j] = __builtin_amdgcn_exp2f(fmaf(S[T][j >> 2][j & 3], C2, -msub)); lsum += p[j]; }
      P[T] = v4u{cvtpk(p[0], p[1]), cvtpk(p[2], p[3]), cvtpk(p[4], p[5]), cvtpk(p[6], p[7])};
    }
    lsum = x32_sum(x16_sum(lsum));
    if (lane < 8) lds[L_SUM + lane * 8 + wv] = lsum;
  }
  LDS_BARRIER();
  if (tid < 16) {
    const f32x4 l0 = *(const f32x4*)(lds + L_SUM + (lane & 7) * 8), l1 = *(const f32x4*)(lds + L_SUM + (lane & 7) * 8 + 4);
    const float L = ((l0[0] + l0[1]) + (l0[2] + l0[3])) + ((l1[0] + l1[1]) + (l1[2] + l1[3]));
    const unsigned u = __float_as_uint(lane < 8 ? M : L);
    __builtin_amdgcn_raw_buffer_store_b128(v4u{(u & 0xFFFFu) | (t48_lo << 16), t48_hi, (u >> 16) | (t48_lo << 16), t48_hi}, wsR,
                                           WS_ML + ((b * 4 + G) * 32 + sp) * 256 + (lane & 7) * 32 + (lane >> 3) * 16, 0, AUX_SC1);
  }
  SB();
  STEP();
  LOADV(2, 1);
  f32x4 O[8];
#pragma unroll
  for (int e = 0; e < 8; ++e) O[e] = f32x4{0.f, 0.f, 0.f, 0.f};
#define PV(T) do { _Pragma("unroll") for (int e_ = 0; e_ < 8; ++e_) { \
      const unsigned sel_ = (e_ & 1) ? 0x07060302u : 0x05040100u; v4u a_; \
      _Pragma("unroll") for (int d_ = 0; d_ < 4; ++d_) a_[d_] = __builtin_amdgcn_perm(V[T][2 * d_ + 1][e_ >> 1], V[T][2 * d_][e_ >> 1], sel_); \
      O[e_] = mfma(a_, P[T], O[e_]); } SB(); } while (0)
#define PV16(t) do { _Pragma("unroll") for (int e_ = 0; e_ < 8; ++e_) { const unsigned sel_ = (e_ & 1) ? 0x07060302u : 0x05040100u; \
      O[e_] = mfma16(v2u{__builtin_amdgcn_perm(V[3][4 * (t) + 1][e_ >> 1], V[3][4 * (t)][e_ >> 1], sel_), __builtin_amdgcn_perm(V[3][4 * (t) + 3][e_ >> 1], V[3][4 * (t) + 2][e_ >> 1], sel_)}, \
                      v2u{P[3][2 * (t)], P[3][2 * (t) + 1]}, O[e_]); } SB(); } while (0)
  PV(0);
  STEP();
  LOADV(3, 0);
  PV(1);
  STEP();
  LOADV(3, 1);
  STEP();
  const unsigned off_mg = WS_ML + ((b * 4 + G) * 32 + (lane >> 1)) * 256 + (wv & 3) * 64 + (lane & 1) * 16;
  v4u ml[2] = {v4u{0u, 0u, 0u, 0u}, v4u{0u, 0u, 0u, 0u}};
  if (wv < 4) { ml[0] = LD128(off_mg); ml[1] = LD128(off_mg + 32); }
  SB();
  PV(2);
  const unsigned off_fl = WS_FL + (G * 64 + idx) * 128;
  if (wv == 7) VMWAIT(4);
  SB();
  if (tid == 448) __builtin_amdgcn_raw_buffer_store_b64(v2u{now32(), t32}, wsR, off_fl, 0, AUX_SC1);
  SB();
  PV16(0);
  if (wv == 7) VMWAIT(1);
  asm volatile("" :: "v"(V[3][7]));
  SB();
  if (tid == 448) __builtin_amdgcn_raw_buffer_store_b64(v2u{now32(), t32}, wsR, off_fl + 8, 0, AUX_SC1);
  SB();
  PV16(1);
  const unsigned off_fg = WS_FL + (G * 64 + lane) * 128;
  v4u fl = {0u, 0u, 0u, 0u};
  if (wv == 0) fl = LD128(off_fg);
  if (c < 8) {
    float* dst = lds + L_RED + (wv * 8 + c) * RED_STRIDE + 32 * g;
#pragma unroll
    for (int i = 0; i < 4; ++i) {
      *(f32x4*)(dst + 8 * i) = f32x4{O[0][i], O[1][i], O[2][i], O[3][i]};
      *(f32x4*)(dst + 8 * i + 4) = f32x4{O[4][i], O[5][i], O[6][i], O[7][i]};
    }
  }
  if (wv < 4) {
    if (!__all((Q_BAD(ml[0]) | Q_BAD(ml[1])) == 0u)) {
      unsigned spins = 0;
      do {
        __builtin_amdgcn_s_sleep(2); RELOAD();
        ml[0] = LD128(off_mg); ml[1] = LD128(off_mg + 32);
      } while (!__all((Q_BAD(ml[0]) | Q_BAD(ml[1])) == 0u) && ++spins < SPIN_CAP);
    }
#pragma unroll
    for (int k = 0; k < 2; ++k) {
      const float v = __uint_as_float((ml[k].x & 0xFFFFu) | (ml[k].z << 16)), vo = dpp<XOR1>(v);
      const float ms = (lane & 1) ? vo : v, ls = (lane & 1) ? v : vo;
      float Mg = fmaxf(ms, dpp<ROR2>(ms)); Mg = fmaxf(Mg, dpp<ROR4>(Mg)); Mg = fmaxf(Mg, dpp<ROR8>(Mg)); Mg = x32_max(x16_max(Mg));
      const float wgt = (ls > 0.f) ? __builtin_amdgcn_exp2f(ms - Mg) : 0.f;
      float Lt = wgt * ls; Lt += dpp<ROR2>(Lt); Lt += dpp<ROR4>(Lt); Lt += dpp<ROR8>(Lt); Lt = x32_sum(x16_sum(Lt));
      if (lane == 2 * sp) lds[L_WGT + wv * 2 + k] = wgt / Lt;
    }
  }
  LDS_BARRIER();
  if (wv >= 4) {
    const int h = (tid >> 5) & 7, d4 = tid & 31;
    f32x4 a = *(const f32x4*)(lds + L_RED + h * RED_STRIDE + 4 * d4);
#pragma unroll
    for (int w = 1; w < 8; ++w) a += *(const f32x4*)(lds + L_RED + (w * 8 + h) * RED_STRIDE + 4 * d4);
    a *= lds[L_WGT + h];
    __builtin_amdgcn_raw_buffer_store_b128(tag4(a, t12), wsR, WS_PO + ((G * 2 + b) * 32 + sp) * 4096 + (tid & 255) * 16, 0, AUX_SC1);
  }
#define FL_BAD (fl.y != t32 && fl.w != t32)
  if (wv == 0) {
    unsigned spins = 0;
    while (__any(FL_BAD && (lane >> 5) == b) && ++spins < SPIN_CAP) {
      __builtin_amdgcn_s_sleep(4); RELOAD();
      if (FL_BAD) fl = LD128(off_fg);
    }
    if (lane == 0) lds_word_store(lds + L_T + 1, 1u);
  } else {
    while (lds_word_load(lds + L_T + 1) == 0u) __builtin_amdgcn_s_sleep(1);
  }
  v4u wov[4];
  {
    const __amdgpu_buffer_rsrc_t woR = __builtin_amdgcn_make_buffer_rsrc((void*)(wo + (size_t)((G * 8 + hB) * 128 + dq * 32) * 1024 + jh * 512 + wv * 64), 0, 31 * 2048 + 128, 0x00020000);
#pragma unroll
    for (int i = 0; i < 4; ++i) wov[i] = __builtin_amdgcn_raw_buffer_load_b128(woR, (lane >> 3) * 2048 + (lane & 7) * 16, i * 16384, AUX_NT);
    SB();
  }
  v4u WA[4];
#define STAGE_WO() do { float* wt = lds + L_WOT + wv * 32 * WOT_STRIDE; \
    _Pragma("unroll") for (int i = 0; i < 4; ++i) *(v4u*)(wt + (8 * i + (lane >> 3)) * WOT_STRIDE + (lane & 7) * 4) = wov[i]; \
    _Pragma("unroll") for (int u = 0; u < 4; ++u) { \
      const float* p0 = wt + (8 * g + (c >> 2)) * WOT_STRIDE + u * 8 + (c & 3) * 2; \
      const v2u lo = tr_read(p0), hi = tr_read(p0 + 4 * WOT_STRIDE); \
      WA[u] = v4u{lo.x, lo.y, hi.x, hi.y}; } SB(); } while (0)
#define LD_FA(k) __builtin_amdgcn_raw_buffer_load_b64(wsR, WS_FL + 8 + lane * 128, (k) * 8192, AUX_SC1)
  v2u fa[4];
  if (wv >= 4) {
    STAGE_WO();
    if (wv == 4) {
#pragma unroll
      for (int k = 0; k < 4; ++k) fa[k] = LD_FA(k);
      asm volatile("" :: "v"(fa[0]), "v"(fa[1]), "v"(fa[2]), "v"(fa[3]));
    }
  } else {
    unsigned spins = 0, t_read;
    if (wv == 0) {
      while (__any(FL_BAD) && ++spins < SPIN_CAP) {
        __builtin_amdgcn_s_sleep(4); RELOAD();
        if (FL_BAD) fl = LD128(off_fg);
      }
      const unsigned ref = now32();
      t_read = (ref + (unsigned)min(wave_max_i32(fl.w == t32 ? (int)(fl.z - ref) + D_PO : (int)(fl.x - ref) + D_PO1), 500)) | 1u;
      if (lane == 0) lds_word_store(lds + L_T, t_read);
      spins = 0;
    } else {
      while ((t_read = lds_word_load(lds + L_T)) == 0u) __builtin_amdgcn_s_sleep(1);
    }
    { const unsigned ref = now32(); wait_until(ref, (int)(t_read - ref)); }
    const unsigned off_pg = WS_PO + ((G * 2 + (wv >> 1)) * 32 + (lane >> 2)) * 4096 + hB * 512 + dq * 128 + (wv & 1) * 64 + (lane & 3) * 16;
    v4u pa[2];
    pa[0] = LD128(off_pg);
    pa[1] = LD128(off_pg + 65536);
    SB();
    STAGE_WO();
    for (;;) {
      const bool bad0 = bad4(pa[0], t12), bad1 = bad4(pa[1], t12);
      if (!__any(bad0 | bad1) || ++spins > SPIN_CAP) break;
      RELOAD();
      if (bad0) pa[0] = LD128(off_pg);
      if (bad1) pa[1] = LD128(off_pg + 65536);
    }
    float o[4];
#pragma unroll
    for (int i = 0; i < 4; ++i) {
      float t = val(pa[0][i]) + val(pa[1][i]);
      t += dpp<ROR4>(t); t += dpp<ROR8>(t);
      o[i] = x32_sum(x16_sum(t));
    }
    if (lane < 4) *(v2u*)(lds + L_OB + (wv >> 1) * 16 + (wv & 1) * 8 + lane * 2) = v2u{cvtpk(o[0], o[1]), cvtpk(o[2], o[3])};
  }
#define FA_BAD (((fa[0].y ^ t32) | (fa[1].y ^ t32) | (fa[2].y ^ t32) | (fa[3].y ^ t32)) != 0u)
#define FA_MAX wave_max_i32(max(max((int)(fa[0].x - fa_ref), (int)(fa[1].x - fa_ref)), max((int)(fa[2].x - fa_ref), (int)(fa[3].x - fa_ref))))
  int fa_max = 0; bool fa_all = false; unsigned fa_ref = 0;
  if (wv == 4) {
    fa_all = !__any(FA_BAD);
    fa_ref = now32();
    fa_max = FA_MAX;
  }
  LDS_BARRIER();
  {
    const v4u ob = *(const v4u*)(lds + L_OB + (c & 1) * 16 + g * 4);
    f32x4 d = {0.f, 0.f, 0.f, 0.f};
#pragma unroll
    for (int u = 0; u < 4; ++u) {
      const bool on = (c >> 1) == u;
      d = mfma(WA[u], v4u{on ? ob.x : 0u, on ? ob.y : 0u, on ? ob.z : 0u, on ? ob.w : 0u}, d);
    }
    const unsigned off_f = WS_F + ((jh * 32 + wv * 4 + ((c >> 1) & 3)) * 128 + (G * 8 + hB) * 4 + dq) * 128 + (c & 1) * 64 + g * 16;
    if (c < 8) __builtin_amdgcn_raw_buffer_store_b128(tag4(d, t12), wsR, off_f, 0, AUX_SC1);
  }
  if (wv == 4) {
    unsigned spins = 0;
    if (!fa_all) {
      do {
        __builtin_amdgcn_s_sleep(4); RELOAD();
#pragma unroll
        for (int k = 0; k < 4; ++k) if (fa[k].y != t32) fa[k] = LD_FA(k);
      } while (__any(FA_BAD) && ++spins < SPIN_CAP);
      fa_ref = now32();
      fa_max = FA_MAX;
    }
    wait_until(fa_ref, fa_max + D_F);
    const int cq = xcd * 32 + sp;
    const unsigned off = WS_F + (cq >> 2) * 16384 + (lane >> 1) * 128 + (lane & 1) * 64 + (cq & 3) * 16;
    v4u f[4];
#pragma unroll
    for (int k = 0; k < 4; ++k) f[k] = LD128(off + k * 4096);
    spins = 0;
    for (;;) {
      bool bad[4];
#pragma unroll
      for (int k = 0; k < 4; ++k) bad[k] = bad4(f[k], t12);
      if (!__any(bad[0] | bad[1] | bad[2] | bad[3]) || ++spins > SPIN_CAP) break;
      RELOAD();
#pragma unroll
      for (int k = 0; k < 4; ++k) if (bad[k]) f[k] = LD128(off + k * 4096);
    }
    float s[4];
#pragma unroll
    for (int i = 0; i < 4; ++i) {
      float t = (val(f[0][i]) + val(f[1][i])) + (val(f[2][i]) + val(f[3][i]));
      t += dpp<ROR2>(t); t += dpp<ROR4>(t); t += dpp<ROR8>(t);
      s[i] = x32_sum(x16_sum(t));
    }
    if (lane < 2) *(v2u*)(out + lane * 1024 + cq * 4) = v2u{cvtpk(s[0], s[1]), cvtpk(s[2], s[3])};
    if (wg == 0 && lane == 0) __builtin_amdgcn_raw_buffer_store_b64(v2u{(unsigned)tag, (unsigned)(tag >> 32)}, wsR, WS_EPOCH, 0, AUX_SC1);
  }
}

extern "C" void kernel_launch(void* const* d_in, const int* in_sizes, int n_in,
                              void* d_out, int out_size, void* d_ws, size_t ws_size,
                              hipStream_t stream) {
  (void)in_sizes; (void)n_in; (void)out_size; (void)ws_size;
  gqa_decode_fused<<<NWG, NT, 0, stream>>>((const u16*)d_in[0], (const u16*)d_in[1], (const u16*)d_in[2],
                                           (const u16*)d_in[3], (const u16*)d_in[4], (u16*)d_out, (unsigned char*)d_ws);
}
```

```cpp
#include <hip/hip_runtime.h>
#include <hip/hip_bf16.h>
#include <stdint.h>

typedef unsigned v4u __attribute__((ext_vector_type(4)));
typedef unsigned v3u __attribute__((ext_vector_type(3)));
typedef unsigned v2u __attribute__((ext_vector_type(2)));
typedef float f32x4 __attribute__((ext_vector_type(4)));
typedef float f32x2 __attribute__((ext_vector_type(2)));
typedef short bf16x8 __attribute__((ext_vector_type(8)));
typedef short v4s __attribute__((ext_vector_type(4)));
typedef __bf16 bf16x2 __attribute__((ext_vector_type(2)));
typedef unsigned short u16;
typedef __attribute__((address_space(3))) unsigned lds_u32;

constexpr int AUX_NT = 2, AUX_SC1 = 16;
constexpr int SK = 32768, DH = 128;
constexpr int NWG = 256, NT = 512;
constexpr unsigned SPIN_CAP = 1u << 16;
constexpr int D_PO = 95, D_PO1 = 100, D_F = 290;

constexpr unsigned WS_EPOCH = 0;
constexpr unsigned WS_QP = 4096;
constexpr unsigned WS_Q = WS_QP + 262144;
constexpr unsigned WS_ML = WS_Q + 65536;
constexpr unsigned WS_PO = WS_ML + 65536;
constexpr unsigned WS_F = WS_PO + (1u << 20);
constexpr unsigned WS_FL = WS_F + (1u << 20);
constexpr unsigned WS_END = WS_FL + 32768;

constexpr int RED_STRIDE = 132, WOT_STRIDE = 36;
constexpr int L_RED = 0;
constexpr int L_WOT = L_RED + 8 * 8 * RED_STRIDE;
constexpr int L_Q = L_WOT + 8 * 32 * WOT_STRIDE;
constexpr int L_QR = L_Q + 512;
constexpr int L_MAX = L_QR + 1024;
constexpr int L_SUM = L_MAX + 64;
constexpr int L_OB = L_SUM + 64;
constexpr int L_T = L_OB + 32;
constexpr int L_TOTAL = L_T + 4;

template <int CTRL> __device__ __forceinline__ float dpp(float x) {
  return __builtin_bit_cast(float, __builtin_amdgcn_mov_dpp(__builtin_bit_cast(int, x), CTRL, 0xf, 0xf, true));
}
constexpr int ROR2 = 0x122, ROR4 = 0x124, ROR8 = 0x128, QUAD0 = 0x00, QUAD1 = 0x55;
__device__ __forceinline__ float x16_sum(float x) {
  auto s = __builtin_amdgcn_permlane16_swap(__float_as_uint(x), __float_as_uint(x), false, false);
  return __uint_as_float(s[0]) + __uint_as_float(s[1]);
}
__device__ __forceinline__ float x16_max(float x) {
  auto s = __builtin_amdgcn_permlane16_swap(__float_as_uint(x), __float_as_uint(x), false, false);
  return fmaxf(__uint_as_float(s[0]), __uint_as_float(s[1]));
}
__device__ __forceinline__ float x32_sum(float x) {
  auto t = __builtin_amdgcn_permlane32_swap(__float_as_uint(x), __float_as_uint(x), false, false);
  return __uint_as_float(t[0]) + __uint_as_float(t[1]);
}
__device__ __forceinline__ float x32_max(float x) {
  auto t = __builtin_amdgcn_permlane32_swap(__float_as_uint(x), __float_as_uint(x), false, false);
  return fmaxf(__uint_as_float(t[0]), __uint_as_float(t[1]));
}
__device__ __forceinline__ float dot2(unsigned a, unsigned b, float c) {
  return __builtin_amdgcn_fdot2_f32_bf16(__builtin_bit_cast(bf16x2, a), __builtin_bit_cast(bf16x2, b), c, false);
}
__device__ __forceinline__ unsigned cvtpk(float lo, float hi) {
  return __builtin_bit_cast(unsigned, __builtin_convertvector(f32x2{lo, hi}, bf16x2));
}
__device__ __forceinline__ f32x4 mfma16(v2u a, v2u b, f32x4 c) {
  return __builtin_amdgcn_mfma_f32_16x16x16bf16_1k(__builtin_bit_cast(v4s, a), __builtin_bit_cast(v4s, b), c, 0, 0, 0);
}
__device__ __forceinline__ f32x4 mfma(v4u a, v4u b, f32x4 c) {
  return __builtin_amdgcn_mfma_f32_16x16x32_bf16(__builtin_bit_cast(bf16x8, a), __builtin_bit_cast(bf16x8, b), c, 0, 0, 0);
}
__device__ __forceinline__ v4u tag4(f32x4 a, v4u t) {
  return v4u{((__float_as_uint(a[0]) + 0x800u) & 0xFFFFF000u) | t.x, ((__float_as_uint(a[1]) + 0x800u) & 0xFFFFF000u) | t.y,
             ((__float_as_uint(a[2]) + 0x800u) & 0xFFFFF000u) | t.z, ((__float_as_uint(a[3]) + 0x800u) & 0xFFFFF000u) | t.w};
}
__device__ __forceinline__ bool bad4(v4u v, v4u t) { return (((v.x ^ t.x) | (v.y ^ t.y) | (v.z ^ t.z) | (v.w ^ t.w)) & 0xFFFu) != 0u; }
__device__ __forceinline__ float val(unsigned u) { return __uint_as_float(u & 0xFFFFF000u); }
__device__ __forceinline__ v2u tr_read(const float* p) {
  return __builtin_bit_cast(v2u, __builtin_amdgcn_ds_read_tr16_b64_v4i16((__attribute__((address_space(3))) v4s*)p));
}
__device__ __forceinline__ void lds_word_store(float* p, unsigned v) { __hip_atomic_store((lds_u32*)p, v, __ATOMIC_RELAXED, __HIP_MEMORY_SCOPE_WORKGROUP); }
__device__ __forceinline__ unsigned lds_word_load(float* p) { return __hip_atomic_load((lds_u32*)p, __ATOMIC_RELAXED, __HIP_MEMORY_SCOPE_WORKGROUP); }
__device__ __forceinline__ unsigned now32() { return (unsigned)__builtin_amdgcn_s_memrealtime(); }
__device__ __forceinline__ int wave_max_i32(int x) {
  x = max(x, __builtin_amdgcn_mov_dpp(x, 0x121, 0xf, 0xf, true)); x = max(x, __builtin_amdgcn_mov_dpp(x, 0x122, 0xf, 0xf, true));
  x = max(x, __builtin_amdgcn_mov_dpp(x, 0x124, 0xf, 0xf, true)); x = max(x, __builtin_amdgcn_mov_dpp(x, 0x128, 0xf, 0xf, true));
  auto s = __builtin_amdgcn_permlane16_swap((unsigned)x, (unsigned)x, false, false); x = max((int)s[0], (int)s[1]);
  auto t = __builtin_amdgcn_permlane32_swap((unsigned)x, (unsigned)x, false, false); x = max((int)t[0], (int)t[1]);
  return __builtin_amdgcn_readfirstlane(x);
}
__device__ __forceinline__ void wait_until(unsigned ref, int dt) {
  dt = min(dt, 500);
  while ((int)(now32() - ref) < dt) {}
}
#define SB() __builtin_amdgcn_sched_barrier(0)
#define RELOAD() asm volatile("" ::: "memory")
#define LDS_BARRIER() do { asm volatile("s_waitcnt lgkmcnt(0)" ::: "memory"); __builtin_amdgcn_s_barrier(); asm volatile("" ::: "memory"); } while (0)
#define VMWAIT(n) asm volatile("s_waitcnt vmcnt(" #n ")" ::: "memory")
#define STEP() do { if (wv == 7) VMWAIT(8); __builtin_amdgcn_s_barrier(); SB(); } while (0)
#define STEP_LDS() do { if (wv == 7) VMWAIT(8); LDS_BARRIER(); SB(); } while (0)
#define LD128(off) __builtin_amdgcn_raw_buffer_load_b128(wsR, (off), 0, AUX_SC1)

__global__ void __launch_bounds__(NT, 2)
gqa_decode_fused(const u16* __restrict__ x, const u16* __restrict__ kc, const u16* __restrict__ vc,
                 const u16* __restrict__ wq, const u16* __restrict__ wo, u16* __restrict__ out, unsigned char* ws) {
  __shared__ __attribute__((aligned(16))) float lds[L_TOTAL];
  const int tid = threadIdx.x, lane = tid & 63, wv = __builtin_amdgcn_readfirstlane(tid >> 6);
  const int wg = blockIdx.x, xcd = wg & 7, G = xcd >> 1, b = xcd & 1, sp = wg >> 3, idx = b * 32 + sp;
  const int cb = sp >> 1, kq = b * 2 + (sp & 1);
  const int hB = idx >> 3, dq = (idx >> 1) & 3, jh = idx & 1;
  const int g = lane >> 4, c = lane & 15;

  constexpr unsigned US = 2u << 20;
  const size_t kvoff = (size_t)(b * 4 + G) * SK * DH * 2 + (sp * 8 + wv) * 8192u;
  const __amdgpu_buffer_rsrc_t kR = __builtin_amdgcn_make_buffer_rsrc((void*)((const unsigned char*)kc + kvoff), 0, 3 * US + 8192, 0x00020000);
  const __amdgpu_buffer_rsrc_t vR = __builtin_amdgcn_make_buffer_rsrc((void*)((const unsigned char*)vc + kvoff), 0, 3 * US + 8192, 0x00020000);
  const __amdgpu_buffer_rsrc_t wsR = __builtin_amdgcn_make_buffer_rsrc((void*)ws, 0, WS_END, 0x00020000);

  v4u K[4][2][4];
  v4u V[4][8];
  const int voK = (4 * (c & 3) + ((c >> 2) & 1)) * 256 + (c >> 3) * 64 + g * 16;
  const int voV = lane * 16;
#define LOADK(T, t) do { _Pragma("unroll") for (int p_ = 0; p_ < 4; ++p_) K[T][t][p_] = __builtin_amdgcn_raw_buffer_load_b128(kR, voK + (p_ & 1) * 128 + (p_ >> 1) * 512, (T) * US + (t) * 4096, AUX_NT); SB(); } while (0)
#define LOADV(T, t) do { _Pragma("unroll") for (int j_ = 0; j_ < 4; ++j_) V[T][(t) * 4 + j_] = __builtin_amdgcn_raw_buffer_load_b128(vR, voV + j_ * 1024, (T) * US + (t) * 4096, AUX_NT); SB(); } while (0)

  v4u wqv[6];
  v3u xv[2];
  {
    const int nr = b ? 2 : 6, row0 = (b ? 768 + (sp & 1) * 128 : (sp & 1) * 384) + wv * 8 * nr;
    const __amdgpu_buffer_rsrc_t wqR = __builtin_amdgcn_make_buffer_rsrc((void*)(wq + (size_t)row0 * 4096 + G * 1024 + cb * 64), 0, (8 * nr - 1) * 8192 + 128, 0x00020000);
    const __amdgpu_buffer_rsrc_t xR = __builtin_amdgcn_make_buffer_rsrc((void*)x, 0, 4096, 0x00020000);
    const int vo = (lane >> 3) * nr * 8192 + (lane & 7) * 16, xo = (row0 + nr * (lane >> 3)) * 2;
    wqv[0] = __builtin_amdgcn_raw_buffer_load_b128(wqR, vo, 0, AUX_NT);
    wqv[1] = __builtin_amdgcn_raw_buffer_load_b128(wqR, vo, 8192, AUX_NT);
    if (b == 0) {
#pragma unroll
      for (int i = 2; i < 6; ++i) wqv[i] = __builtin_amdgcn_raw_buffer_load_b128(wqR, vo, i * 8192, AUX_NT);
      xv[0] = __builtin_amdgcn_raw_buffer_load_b96(xR, xo, 0, AUX_NT);
      xv[1] = __builtin_amdgcn_raw_buffer_load_b96(xR, 2048 + xo, 0, AUX_NT);
    } else {
#pragma unroll
      for (int i = 2; i < 6; ++i) wqv[i] = v4u{0u, 0u, 0u, 0u};
      xv[0] = v3u{__builtin_amdgcn_raw_buffer_load_b32(xR, xo, 0, AUX_NT), 0u, 0u};
      xv[1] = v3u{__builtin_amdgcn_raw_buffer_load_b32(xR, 2048 + xo, 0, AUX_NT), 0u, 0u};
    }
  }
  const v2u ep = __builtin_amdgcn_raw_buffer_load_b64(wsR, WS_EPOCH, 0, AUX_SC1);
  SB();
  __builtin_amdgcn_s_barrier();
  SB();
  LOADK(0, 0); LOADK(0, 1);
  {
    float qa[2][8];
#pragma unroll
    for (int e = 0; e < 8; ++e) {
      const unsigned sel = (e & 1) ? 0x07060302u : 0x05040100u;
      const unsigned p01 = __builtin_amdgcn_perm(wqv[1][e >> 1], wqv[0][e >> 1], sel);
      const unsigned p23 = __builtin_amdgcn_perm(wqv[3][e >> 1], wqv[2][e >> 1], sel);
      const unsigned p45 = __builtin_amdgcn_perm(wqv[5][e >> 1], wqv[4][e >> 1], sel);
#pragma unroll
      for (int bb = 0; bb < 2; ++bb) {
        float t = dot2(p45, xv[bb].z, dot2(p23, xv[bb].y, dot2(p01, xv[bb].x, 0.f)));
        t += dpp<ROR8>(t);
        qa[bb][e] = x32_sum(x16_sum(t));
      }
    }
    if (lane < 8) {
      float* dst = lds + L_QR + wv * 128 + lane * 8;
#pragma unroll
      for (int bb = 0; bb < 2; ++bb) {
        *(f32x4*)(dst + bb * 64) = f32x4{qa[bb][0], qa[bb][1], qa[bb][2], qa[bb][3]};
        *(f32x4*)(dst + bb * 64 + 4) = f32x4{qa[bb][4], qa[bb][5], qa[bb][6], qa[bb][7]};
      }
    }
    SB();
  }
  const unsigned long long tag = (((unsigned long long)__builtin_amdgcn_readfirstlane(ep.y) << 32) | __builtin_amdgcn_readfirstlane(ep.x)) + 1ull;
  const unsigned t32 = (unsigned)tag;
  const unsigned t40_lo = (unsigned)tag & 0xFFu, t40_hi = (unsigned)(tag >> 8);
  const unsigned t48_lo = (unsigned)tag & 0xFFFFu, t48_hi = (unsigned)(tag >> 16);
  const unsigned long long tw = (tag & 0xFFFFFFFFFFFFull) * 0x001001001001ull;
  const v4u t12 = {(unsigned)tag & 0xFFFu, (unsigned)(tw >> 12) & 0xFFFu, (unsigned)(tw >> 24) & 0xFFFu, (unsigned)(tw >> 36) & 0xFFFu};
  if (tid == 0) { lds_word_store(lds + L_T, 0u); lds_word_store(lds + L_T + 1, 0u); }
  LDS_BARRIER();
  if (tid < 128) {
    float s0 = 0.f;
#pragma unroll
    for (int w = 0; w < 8; ++w) s0 += lds[L_QR + w * 128 + tid];
    const unsigned off = WS_QP + (((G * 16 + cb) * 4 + ((tid >> 4) & 3)) * 4 + kq) * 256 + (tid >> 6) * 128 + (tid & 15) * 8;
    __builtin_amdgcn_raw_buffer_store_b64(v2u{(__float_as_uint(s0) & 0xFFFFFF00u) | t40_lo, t40_hi}, wsR, off, 0, AUX_SC1);
  }
  SB();
  LOADK(1, 0);
  STEP();
  LOADK(1, 1);
  STEP();
  const unsigned off_qp = WS_QP + ((G * 16 + cb) * 4 + kq) * 1024 + lane * 16;
  v4u qp = {0u, 0u, 0u, 0u};
  const bool q_early = wv == 0 && !b, q_late = wv == 0 && b;
  if (q_early) qp = LD128(off_qp);
  SB();
  LOADK(2, 0);
  STEP();
  if (q_late) qp = LD128(off_qp);
  SB();
  LOADK(2, 1);
  STEP();
  LOADK(3, 0);
#define QP_BAD(v) ((((v).x ^ t40_lo) & 0xFFu) | ((v).y ^ t40_hi) | (((v).z ^ t40_lo) & 0xFFu) | ((v).w ^ t40_hi))
#define HOP2() do { \
    if (!__all(QP_BAD(qp) == 0u)) { \
      unsigned spins = 0; \
      do { __builtin_amdgcn_s_sleep(4); RELOAD(); qp = LD128(off_qp); } while (!__all(QP_BAD(qp) == 0u) && ++spins < SPIN_CAP); \
    } \
    const float q0 = x32_sum(x16_sum(__uint_as_float(qp.x & 0xFFFFFF00u))), q1 = x32_sum(x16_sum(__uint_as_float(qp.z & 0xFFFFFF00u))); \
    const unsigned pk = cvtpk(q0, q1);                   \
    if (lane < 16) __builtin_amdgcn_raw_buffer_store_b128(v4u{(pk & 0xFFFFu) | (t48_lo << 16), t48_hi, (pk >> 16) | (t48_lo << 16), t48_hi}, wsR, \
                                             WS_Q + ((lane >> 3) * 4096 + G * 1024 + (cb * 4 + kq) * 16 + (lane & 7) * 2) * 8, 0, AUX_SC1); \
    SB(); } while (0)
  if (q_early) HOP2();
  STEP();
  LOADK(3, 1);
  if (q_late) HOP2();
  STEP();
  LOADV(0, 0);
  STEP();
  const unsigned off_q = WS_Q + (b * 4096 + G * 1024 + ((wv - 1) & 3) * 256) * 8 + lane * 16;
  v4u qg[2] = {v4u{0u, 0u, 0u, 0u}, v4u{0u, 0u, 0u, 0u}};
  if (wv >= 1 && wv <= 4) { qg[0] = LD128(off_q); qg[1] = LD128(off_q + 1024); }
  SB();
  LOADV(0, 1);
  STEP();
  LOADV(1, 0);
  STEP();
  LOADV(1, 1);
#define Q_BAD(v) ((((v).x >> 16) ^ t48_lo) | ((v).y ^ t48_hi) | (((v).z >> 16) ^ t48_lo) | ((v).w ^ t48_hi))
  if (wv >= 1 && wv <= 4) {
    if (!__all((Q_BAD(qg[0]) | Q_BAD(qg[1])) == 0u)) {
      unsigned spins = 0;
      do {
        __builtin_amdgcn_s_sleep(4); RELOAD();
        qg[0] = LD128(off_q); qg[1] = LD128(off_q + 1024);
      } while (!__all((Q_BAD(qg[0]) | Q_BAD(qg[1])) == 0u) && ++spins < SPIN_CAP);
    }
    ((unsigned*)lds)[L_Q + (wv - 1) * 128 + lane] = (qg[0].x & 0xFFFFu) | (qg[0].z << 16);
    ((unsigned*)lds)[L_Q + (wv - 1) * 128 + 64 + lane] = (qg[1].x & 0xFFFFu) | (qg[1].z << 16);
  }
  LDS_BARRIER();
  v4u Q[2];
#pragma unroll
  for (int h = 0; h < 2; ++h) Q[h] = *(const v4u*)(lds + L_Q + (c & 7) * 64 + h * 32 + (c >> 3) * 16 + g * 4);
  f32x4 S[4][2];
#define QK(T) do { _Pragma("unroll") for (int t_ = 0; t_ < 2; ++t_) { const f32x4 z_ = {0.f, 0.f, 0.f, 0.f}; \
    const f32x4 dx_ = mfma(K[T][t_][1], Q[1], mfma(K[T][t_][0], Q[0], z_)), dy_ = mfma(K[T][t_][3], Q[1], mfma(K[T][t_][2], Q[0], z_)); \
    _Pragma("unroll") for (int i_ = 0; i_ < 4; ++i_) { \
      auto sw_ = __builtin_amdgcn_permlane32_swap(__float_as_uint(dx_[i_]), __float_as_uint(dy_[i_]), false, false); \
      S[T][t_][i_] = __uint_as_float(sw_[0]) + dpp<ROR8>(__uint_as_float(sw_[1])); } } SB(); } while (0)
  QK(0); QK(1); QK(2); QK(3);
  constexpr float C2 = 0.08838834764831845f * 1.4426950408889634f;
  float mx = -INFINITY;
#pragma unroll
  for (int T = 0; T < 4; ++T)
#pragma unroll
    for (int t = 0; t < 2; ++t) mx = fmaxf(mx, fmaxf(fmaxf(S[T][t][0], S[T][t][1]), fmaxf(S[T][t][2], S[T][t][3])));
  mx = x32_max(x16_max(mx)) * C2;
  if (lane < 8) lds[L_MAX + lane * 8 + wv] = mx;
  STEP_LDS();
  LOADV(2, 0);
  float M;
  {
    const f32x4 m0 = *(const f32x4*)(lds + L_MAX + (c & 7) * 8), m1 = *(const f32x4*)(lds + L_MAX + (c & 7) * 8 + 4);
    M = fmaxf(fmaxf(fmaxf(m0[0], m0[1]), fmaxf(m0[2], m0[3])), fmaxf(fmaxf(m1[0], m1[1]), fmaxf(m1[2], m1[3])));
    if (c >= 8) M = mx;
  }
  v4u P[4];
  {
    const float msub = (M == -INFINITY) ? 0.f : M;
    float lsum = 0.f;
#pragma unroll
    for (int T = 0; T < 4; ++T) {
      float p[8];
#pragma unroll
      for (int j = 0; j < 8; ++j) { p[j] = __builtin_amdgcn_exp2f(fmaf(S[T][j >> 2][j & 3], C2, -msub)); lsum += p[j]; }
      P[T] = v4u{cvtpk(p[0], p[1]), cvtpk(p[2], p[3]), cvtpk(p[4], p[5]), cvtpk(p[6], p[7])};
    }
    lsum = x32_sum(x16_sum(lsum));
    if (lane < 8) lds[L_SUM + lane * 8 + wv] = lsum;
  }
  LDS_BARRIER();
  if (tid < 16) {
    const f32x4 l0 = *(const f32x4*)(lds + L_SUM + (lane & 7) * 8), l1 = *(const f32x4*)(lds + L_SUM + (lane & 7) * 8 + 4);
    const float L = ((l0[0] + l0[1]) + (l0[2] + l0[3])) + ((l1[0] + l1[1]) + (l1[2] + l1[3]));
    const unsigned u = __float_as_uint(lane < 8 ? M : L);
    __builtin_amdgcn_raw_buffer_store_b128(v4u{(u & 0xFFFFu) | (t48_lo << 16), t48_hi, (u >> 16) | (t48_lo << 16), t48_hi}, wsR,
                                           WS_ML + ((b * 4 + G) * 32 + sp) * 256 + (lane & 7) * 32 + (lane >> 3) * 16, 0, AUX_SC1);
  }
  SB();
  STEP();
  LOADV(2, 1);
  f32x4 O[8];
#pragma unroll
  for (int e = 0; e < 8; ++e) O[e] = f32x4{0.f, 0.f, 0.f, 0.f};
#define PV(T) do { _Pragma("unroll") for (int e_ = 0; e_ < 8; ++e_) { \
      const unsigned sel_ = (e_ & 1) ? 0x07060302u : 0x05040100u; v4u a_; \
      _Pragma("unroll") for (int d_ = 0; d_ < 4; ++d_) a_[d_] = __builtin_amdgcn_perm(V[T][2 * d_ + 1][e_ >> 1], V[T][2 * d_][e_ >> 1], sel_); \
      O[e_] = mfma(a_, P[T], O[e_]); } SB(); } while (0)
#define PV16(t) do { _Pragma("unroll") for (int e_ = 0; e_ < 8; ++e_) { const unsigned sel_ = (e_ & 1) ? 0x07060302u : 0x05040100u; \
      O[e_] = mfma16(v2u{__builtin_amdgcn_perm(V[3][4 * (t) + 1][e_ >> 1], V[3][4 * (t)][e_ >> 1], sel_), __builtin_amdgcn_perm(V[3][4 * (t) + 3][e_ >> 1], V[3][4 * (t) + 2][e_ >> 1], sel_)}, \
                      v2u{P[3][2 * (t)], P[3][2 * (t) + 1]}, O[e_]); } SB(); } while (0)
  PV(0);
  STEP();
  LOADV(3, 0);
  PV(1);
  STEP();
  LOADV(3, 1);
  STEP();
  PV(2);
  const unsigned off_fl = WS_FL + (G * 64 + idx) * 128;
  if (wv == 7) VMWAIT(4);
  SB();
  if (tid == 448) __builtin_amdgcn_raw_buffer_store_b64(v2u{now32(), t32}, wsR, off_fl, 0, AUX_SC1);
  SB();
  PV16(0);
  if (wv == 7) VMWAIT(1);
  asm volatile("" :: "v"(V[3][7]));
  SB();
  if (tid == 448) __builtin_amdgcn_raw_buffer_store_b64(v2u{now32(), t32}, wsR, off_fl + 8, 0, AUX_SC1);
  SB();
  PV16(1);
  const unsigned off_fg = WS_FL + (G * 64 + lane) * 128;
  v4u fl = {0u, 0u, 0u, 0u};
  if (wv == 0) fl = LD128(off_fg);
  if (c < 8) {
    float* dst = lds + L_RED + (wv * 8 + c) * RED_STRIDE + 32 * g;
#pragma unroll
    for (int i = 0; i < 4; ++i) {
      *(f32x4*)(dst + 8 * i) = f32x4{O[0][i], O[1][i], O[2][i], O[3][i]};
      *(f32x4*)(dst + 8 * i + 4) = f32x4{O[4][i], O[5][i], O[6][i], O[7][i]};
    }
  }
  const unsigned off_mg = WS_ML + (((wv >> 1) * 4 + G) * 32 + (lane >> 2)) * 256 + hB * 32 + (lane & 1) * 16;
  v4u ml[2] = {v4u{0u, 0u, 0u, 0u}, v4u{0u, 0u, 0u, 0u}};
  if (wv < 4) { ml[0] = LD128(off_mg); ml[1] = LD128(off_mg + 4096); }
  LDS_BARRIER();
  if (wv >= 4) {
    const int h = (tid >> 5) & 7, d4 = tid & 31;
    f32x4 a = *(const f32x4*)(lds + L_RED + h * RED_STRIDE + 4 * d4);
#pragma unroll
    for (int w = 1; w < 8; ++w) a += *(const f32x4*)(lds + L_RED + (w * 8 + h) * RED_STRIDE + 4 * d4);
    __builtin_amdgcn_raw_buffer_store_b128(tag4(a, t12), wsR, WS_PO + ((G * 2 + b) * 32 + sp) * 4096 + (tid & 255) * 16, 0, AUX_SC1);
  }
#define FL_BAD (fl.y != t32 && fl.w != t32)
  if (wv == 0) {
    unsigned spins = 0;
    while (__any(FL_BAD && (lane >> 5) == b) && ++spins < SPIN_CAP) {
      __builtin_amdgcn_s_sleep(4); RELOAD();
      if (FL_BAD) fl = LD128(off_fg);
    }
    if (lane == 0) lds_word_store(lds + L_T + 1, 1u);
  } else {
    while (lds_word_load(lds + L_T + 1) == 0u) __builtin_amdgcn_s_sleep(1);
  }
  v4u wov[4];
  {
    const __amdgpu_buffer_rsrc_t woR = __builtin_amdgcn_make_buffer_rsrc((void*)(wo + (size_t)((G * 8 + hB) * 128 + dq * 32) * 1024 + jh * 512 + wv * 64), 0, 31 * 2048 + 128, 0x00020000);
#pragma unroll
    for (int i = 0; i < 4; ++i) wov[i] = __builtin_amdgcn_raw_buffer_load_b128(woR, (lane >> 3) * 2048 + (lane & 7) * 16, i * 16384, AUX_NT);
    SB();
  }
  v4u WA[4];
#define STAGE_WO() do { float* wt = lds + L_WOT + wv * 32 * WOT_STRIDE; \
    _Pragma("unroll") for (int i = 0; i < 4; ++i) *(v4u*)(wt + (8 * i + (lane >> 3)) * WOT_STRIDE + (lane & 7) * 4) = wov[i]; \
    _Pragma("unroll") for (int u = 0; u < 4; ++u) { \
      const float* p0 = wt + (8 * g + (c >> 2)) * WOT_STRIDE + u * 8 + (c & 3) * 2; \
      const v2u lo = tr_read(p0), hi = tr_read(p0 + 4 * WOT_STRIDE); \
      WA[u] = v4u{lo.x, lo.y, hi.x, hi.y}; } SB(); } while (0)
#define LD_FA(k) __builtin_amdgcn_raw_buffer_load_b64(wsR, WS_FL + 8 + lane * 128, (k) * 8192, AUX_SC1)
  v2u fa[4];
  if (wv >= 4) {
    STAGE_WO();
    if (wv == 4) {
#pragma unroll
      for (int k = 0; k < 4; ++k) fa[k] = LD_FA(k);
      asm volatile("" :: "v"(fa[0]), "v"(fa[1]), "v"(fa[2]), "v"(fa[3]));
    }
  } else {
    float w0, w1;
    {
      if (!__all((Q_BAD(ml[0]) | Q_BAD(ml[1])) == 0u)) {
        unsigned spins = 0;
        do {
          __builtin_amdgcn_s_sleep(4); RELOAD();
          ml[0] = LD128(off_mg); ml[1] = LD128(off_mg + 4096);
        } while (!__all((Q_BAD(ml[0]) | Q_BAD(ml[1])) == 0u) && ++spins < SPIN_CAP);
      }
      const float v0 = __uint_as_float((ml[0].x & 0xFFFFu) | (ml[0].z << 16)), v1 = __uint_as_float((ml[1].x & 0xFFFFu) | (ml[1].z << 16));
      const float M0 = dpp<QUAD0>(v0), L0 = dpp<QUAD1>(v0), M1 = dpp<QUAD0>(v1), L1 = dpp<QUAD1>(v1);
      float Mg = fmaxf(M0, M1); Mg = fmaxf(Mg, dpp<ROR4>(Mg)); Mg = fmaxf(Mg, dpp<ROR8>(Mg)); Mg = x32_max(x16_max(Mg));
      w0 = (L0 > 0.f) ? __builtin_amdgcn_exp2f(M0 - Mg) : 0.f; w1 = (L1 > 0.f) ? __builtin_amdgcn_exp2f(M1 - Mg) : 0.f;
      float Lt = w0 * L0 + w1 * L1; Lt += dpp<ROR4>(Lt); Lt += dpp<ROR8>(Lt); Lt = x32_sum(x16_sum(Lt));
      const float inv = 1.f / Lt;
      w0 *= inv; w1 *= inv;
      SB();
    }
    unsigned spins = 0, t_read;
    if (wv == 0) {
      while (__any(FL_BAD) && ++spins < SPIN_CAP) {
        __builtin_amdgcn_s_sleep(4); RELOAD();
        if (FL_BAD) fl = LD128(off_fg);
      }
      const unsigned ref = now32();
      t_read = (ref + (unsigned)min(wave_max_i32(fl.w == t32 ? (int)(fl.z - ref) + D_PO : (int)(fl.x - ref) + D_PO1), 500)) | 1u;
      if (lane == 0) lds_word_store(lds + L_T, t_read);
      spins = 0;
    } else {
      while ((t_read = lds_word_load(lds + L_T)) == 0u) __builtin_amdgcn_s_sleep(1);
    }
    { const unsigned ref = now32(); wait_until(ref, (int)(t_read - ref)); }
    const unsigned off_pg = WS_PO + ((G * 2 + (wv >> 1)) * 32 + (lane >> 2)) * 4096 + hB * 512 + dq * 128 + (wv & 1) * 64 + (lane & 3) * 16;
    v4u pa[2];
    pa[0] = LD128(off_pg);
    pa[1] = LD128(off_pg + 65536);
    SB();
    STAGE_WO();
    for (;;) {
      const bool bad0 = bad4(pa[0], t12), bad1 = bad4(pa[1], t12);
      if (!__any(bad0 | bad1) || ++spins > SPIN_CAP) break;
      RELOAD();
      if (bad0) pa[0] = LD128(off_pg);
      if (bad1) pa[1] = LD128(off_pg + 65536);
    }
    float o[4];
#pragma unroll
    for (int i = 0; i < 4; ++i) {
      float t = w0 * val(pa[0][i]) + w1 * val(pa[1][i]);
      t += dpp<ROR4>(t); t += dpp<ROR8>(t);
      o[i] = x32_sum(x16_sum(t));
    }
    if (lane < 4) *(v2u*)(lds + L_OB + (wv >> 1) * 16 + (wv & 1) * 8 + lane * 2) = v2u{cvtpk(o[0], o[1]), cvtpk(o[2], o[3])};
  }
#define FA_BAD (((fa[0].y ^ t32) | (fa[1].y ^ t32) | (fa[2].y ^ t32) | (fa[3].y ^ t32)) != 0u)
#define FA_MAX wave_max_i32(max(max((int)(fa[0].x - fa_ref), (int)(fa[1].x - fa_ref)), max((int)(fa[2].x - fa_ref), (int)(fa[3].x - fa_ref))))
  int fa_max = 0; bool fa_all = false; unsigned fa_ref = 0;
  if (wv == 4) {
    fa_all = !__any(FA_BAD);
    fa_ref = now32();
    fa_max = FA_MAX;
  }
  LDS_BARRIER();
  {
    const v4u ob = *(const v4u*)(lds + L_OB + (c & 1) * 16 + g * 4);
    f32x4 d = {0.f, 0.f, 0.f, 0.f};
#pragma unroll
    for (int u = 0; u < 4; ++u) {
      const bool on = (c >> 1) == u;
      d = mfma(WA[u], v4u{on ? ob.x : 0u, on ? ob.y : 0u, on ? ob.z : 0u, on ? ob.w : 0u}, d);
    }
    const unsigned off_f = WS_F + ((jh * 32 + wv * 4 + ((c >> 1) & 3)) * 128 + (G * 8 + hB) * 4 + dq) * 128 + (c & 1) * 64 + g * 16;
    if (c < 8) __builtin_amdgcn_raw_buffer_store_b128(tag4(d, t12), wsR, off_f, 0, AUX_SC1);
  }
  if (wv == 4) {
    unsigned spins = 0;
    if (!fa_all) {
      do {
        __builtin_amdgcn_s_sleep(4); RELOAD();
#pragma unroll
        for (int k = 0; k < 4; ++k) if (fa[k].y != t32) fa[k] = LD_FA(k);
      } while (__any(FA_BAD) && ++spins < SPIN_CAP);
      fa_ref = now32();
      fa_max = FA_MAX;
    }
    wait_until(fa_ref, fa_max + D_F);
    const int cq = xcd * 32 + sp;
    const unsigned off = WS_F + (cq >> 2) * 16384 + (lane >> 1) * 128 + (lane & 1) * 64 + (cq & 3) * 16;
    v4u f[4];
#pragma unroll
    for (int k = 0; k < 4; ++k) f[k] = LD128(off + k * 4096);
    spins = 0;
    for (;;) {
      bool bad[4];
#pragma unroll
      for (int k = 0; k < 4; ++k) bad[k] = bad4(f[k], t12);
      if (!__any(bad[0] | bad[1] | bad[2] | bad[3]) || ++spins > SPIN_CAP) break;
      RELOAD();
#pragma unroll
      for (int k = 0; k < 4; ++k) if (bad[k]) f[k] = LD128(off + k * 4096);
    }
    float s[4];
#pragma unroll
    for (int i = 0; i < 4; ++i) {
      float t = (val(f[0][i]) + val(f[1][i])) + (val(f[2][i]) + val(f[3][i]));
      t += dpp<ROR2>(t); t += dpp<ROR4>(t); t += dpp<ROR8>(t);
      s[i] = x32_sum(x16_sum(t));
    }
    if (lane < 2) *(v2u*)(out + lane * 1024 + cq * 4) = v2u{cvtpk(s[0], s[1]), cvtpk(s[2], s[3])};
    if (wg == 0 && lane == 0) __builtin_amdgcn_raw_buffer_store_b64(v2u{(unsigned)tag, (unsigned)(tag >> 32)}, wsR, WS_EPOCH, 0, AUX_SC1);
  }
}

extern "C" void kernel_launch(void* const* d_in, const int* in_sizes, int n_in,
                              void* d_out, int out_size, void* d_ws, size_t ws_size,
                              hipStream_t stream) {
  (void)in_sizes; (void)n_in; (void)out_size; (void)ws_size;
  gqa_decode_fused<<<NWG, NT, 0, stream>>>((const u16*)d_in[0], (const u16*)d_in[1], (const u16*)d_in[2],
                                           (const u16*)d_in[3], (const u16*)d_in[4], (u16*)d_out, (unsigned char*)d_ws);
}
```

```cpp
#include <hip/hip_runtime.h>
#include <hip/hip_bf16.h>
#include <stdint.h>

typedef unsigned v4u __attribute__((ext_vector_type(4)));
typedef unsigned v3u __attribute__((ext_vector_type(3)));
typedef unsigned v2u __attribute__((ext_vector_type(2)));
typedef float f32x4 __attribute__((ext_vector_type(4)));
typedef float f32x2 __attribute__((ext_vector_type(2)));
typedef short bf16x8 __attribute__((ext_vector_type(8)));
typedef short v4s __attribute__((ext_vector_type(4)));
typedef __bf16 bf16x2 __attribute__((ext_vector_type(2)));
typedef unsigned short u16;
typedef __attribute__((address_space(3))) unsigned lds_u32;

constexpr int AUX_NT = 2, AUX_SC1 = 16;
constexpr int SK = 32768, DH = 128;
constexpr int NWG = 256, NT = 512;
constexpr unsigned SPIN_CAP = 1u << 16;
constexpr int D_PO = 95, D_PO1 = 100, D_F = 290;

constexpr unsigned WS_EPOCH = 0;
constexpr unsigned WS_QP = 4096;
constexpr unsigned WS_Q = WS_QP + 262144;
constexpr unsigned WS_ML = WS_Q + 65536;
constexpr unsigned WS_PO = WS_ML + 65536;
constexpr unsigned WS_F = WS_PO + (1u << 20);
constexpr unsigned WS_FL = WS_F + (1u << 20);
constexpr unsigned WS_END = WS_FL + 32768;

constexpr int RED_STRIDE = 132, WOT_STRIDE = 36;
constexpr int L_RED = 0;
constexpr int L_WOT = L_RED + 8 * 8 * RED_STRIDE;
constexpr int L_Q = L_WOT + 8 * 32 * WOT_STRIDE;
constexpr int L_QR = L_Q + 512;
constexpr int L_MAX = L_QR + 1024;
constexpr int L_SUM = L_MAX + 64;
constexpr int L_OB = L_SUM + 64;
constexpr int L_T = L_OB + 32;
constexpr int L_TOTAL = L_T + 4;

template <int CTRL> __device__ __forceinline__ float dpp(float x) {
  return __builtin_bit_cast(float, __builtin_amdgcn_mov_dpp(__builtin_bit_cast(int, x), CTRL, 0xf, 0xf, true));
}
constexpr int ROR2 = 0x122, ROR4 = 0x124, ROR8 = 0x128, QUAD0 = 0x00, QUAD1 = 0x55;
__device__ __forceinline__ float x16_sum(float x) {
  auto s = __builtin_amdgcn_permlane16_swap(__float_as_uint(x), __float_as_uint(x), false, false);
  return __uint_as_float(s[0]) + __uint_as_float(s[1]);
}
__device__ __forceinline__ float x16_max(float x) {
  auto s = __builtin_amdgcn_permlane16_swap(__float_as_uint(x), __float_as_uint(x), false, false);
  return fmaxf(__uint_as_float(s[0]), __uint_as_float(s[1]));
}
__device__ __forceinline__ float x32_sum(float x) {
  auto t = __builtin_amdgcn_permlane32_swap(__float_as_uint(x), __float_as_uint(x), false, false);
  return __uint_as_float(t[0]) + __uint_as_float(t[1]);
}
__device__ __forceinline__ float x32_max(float x) {
  auto t = __builtin_amdgcn_permlane32_swap(__float_as_uint(x), __float_as_uint(x), false, false);
  return fmaxf(__uint_as_float(t[0]), __uint_as_float(t[1]));
}
__device__ __forceinline__ float dot2(unsigned a, unsigned b, float c) {
  return __builtin_amdgcn_fdot2_f32_bf16(__builtin_bit_cast(bf16x2, a), __builtin_bit_cast(bf16x2, b), c, false);
}
__device__ __forceinline__ unsigned cvtpk(float lo, float hi) {
  return __builtin_bit_cast(unsigned, __builtin_convertvector(f32x2{lo, hi}, bf16x2));
}
__device__ __forceinline__ f32x4 mfma16(v2u a, v2u b, f32x4 c) {
  return __builtin_amdgcn_mfma_f32_16x16x16bf16_1k(__builtin_bit_cast(v4s, a), __builtin_bit_cast(v4s, b), c, 0, 0, 0);
}
__device__ __forceinline__ f32x4 mfma(v4u a, v4u b, f32x4 c) {
  return __builtin_amdgcn_mfma_f32_16x16x32_bf16(__builtin_bit_cast(bf16x8, a), __builtin_bit_cast(bf16x8, b), c, 0, 0, 0);
}
__device__ __forceinline__ v4u tag4(f32x4 a, v4u t) {
  return v4u{((__float_as_uint(a[0]) + 0x800u) & 0xFFFFF000u) | t.x, ((__float_as_uint(a[1]) + 0x800u) & 0xFFFFF000u) | t.y,
             ((__float_as_uint(a[2]) + 0x800u) & 0xFFFFF000u) | t.z, ((__float_as_uint(a[3]) + 0x800u) & 0xFFFFF000u) | t.w};
}
__device__ __forceinline__ bool bad4(v4u v, v4u t) { return (((v.x ^ t.x) | (v.y ^ t.y) | (v.z ^ t.z) | (v.w ^ t.w)) & 0xFFFu) != 0u; }
__device__ __forceinline__ float val(unsigned u) { return __uint_as_float(u & 0xFFFFF000u); }
__device__ __forceinline__ v2u tr_read(const float* p) {
  return __builtin_bit_cast(v2u, __builtin_amdgcn_ds_read_tr16_b64_v4i16((__attribute__((address_space(3))) v4s*)p));
}
__device__ __forceinline__ void lds_word_store(float* p, unsigned v) { __hip_atomic_store((lds_u32*)p, v, __ATOMIC_RELAXED, __HIP_MEMORY_SCOPE_WORKGROUP); }
__device__ __forceinline__ unsigned lds_word_load(float* p) { return __hip_atomic_load((lds_u32*)p, __ATOMIC_RELAXED, __HIP_MEMORY_SCOPE_WORKGROUP); }
__device__ __forceinline__ unsigned now32() { return (unsigned)__builtin_amdgcn_s_memrealtime(); }
__device__ __forceinline__ int wave_max_i32(int x) {
  x = max(x, __builtin_amdgcn_mov_dpp(x, 0x121, 0xf, 0xf, true)); x = max(x, __builtin_amdgcn_mov_dpp(x, 0x122, 0xf, 0xf, true));
  x = max(x, __builtin_amdgcn_mov_dpp(x, 0x124, 0xf, 0xf, true)); x = max(x, __builtin_amdgcn_mov_dpp(x, 0x128, 0xf, 0xf, true));
  auto s = __builtin_amdgcn_permlane16_swap((unsigned)x, (unsigned)x, false, false); x = max((int)s[0], (int)s[1]);
  auto t = __builtin_amdgcn_permlane32_swap((unsigned)x, (unsigned)x, false, false); x = max((int)t[0], (int)t[1]);
  return __builtin_amdgcn_readfirstlane(x);
}
__device__ __forceinline__ void wait_until(unsigned ref, int dt) {
  dt = min(dt, 500);
  while ((int)(now32() - ref) < dt) {}
}
#define SB() __builtin_amdgcn_sched_barrier(0)
#define RELOAD() asm volatile("" ::: "memory")
#define LDS_BARRIER() do { asm volatile("s_waitcnt lgkmcnt(0)" ::: "memory"); __builtin_amdgcn_s_barrier(); asm volatile("" ::: "memory"); } while (0)
#define VMWAIT(n) asm volatile("s_waitcnt vmcnt(" #n ")" ::: "memory")
#define STEP() do { if (wv == 7) VMWAIT(8); __builtin_amdgcn_s_barrier(); SB(); } while (0)
#define STEP_LDS() do { if (wv == 7) VMWAIT(8); LDS_BARRIER(); SB(); } while (0)
#define LD128(off) __builtin_amdgcn_raw_buffer_load_b128(wsR, (off), 0, AUX_SC1)

__global__ void __launch_bounds__(NT, 2)
gqa_decode_fused(const u16* __restrict__ x, const u16* __restrict__ kc, const u16* __restrict__ vc,
                 const u16* __restrict__ wq, const u16* __restrict__ wo, u16* __restrict__ out, unsigned char* ws) {
  __shared__ __attribute__((aligned(16))) float lds[L_TOTAL];
  const int tid = threadIdx.x, lane = tid & 63, wv = __builtin_amdgcn_readfirstlane(tid >> 6);
  const int wg = blockIdx.x, xcd = wg & 7, G = xcd >> 1, b = xcd & 1, sp = wg >> 3, idx = b * 32 + sp;
  const int cb = sp >> 1, kq = b * 2 + (sp & 1);
  const int hB = idx >> 3, dq = (idx >> 1) & 3, jh = idx & 1;
  const int g = lane >> 4, c = lane & 15;

  const int nr = 6 - 4 * b, row0 = b * 768 + (sp & 1) * (384 - 256 * b) + wv * 8 * nr;
  unsigned wq_off = (unsigned)row0 * 8192u + G * 2048 + cb * 128;
  int vo = (lane >> 3) * nr * 8192 + (lane & 7) * 16, xo = (row0 + nr * (lane >> 3)) * 2;
  asm volatile("" : "+s"(wq_off), "+v"(vo), "+v"(xo) : "s"(x), "s"(kc), "s"(vc), "s"(wq), "s"(wo), "s"(out), "s"(ws));

  constexpr unsigned US = 2u << 20;
  const size_t kvoff = (size_t)(b * 4 + G) * SK * DH * 2 + (sp * 8 + wv) * 8192u;
  const __amdgpu_buffer_rsrc_t kR = __builtin_amdgcn_make_buffer_rsrc((void*)((const unsigned char*)kc + kvoff), 0, 3 * US + 8192, 0x00020000);
  const __amdgpu_buffer_rsrc_t vR = __builtin_amdgcn_make_buffer_rsrc((void*)((const unsigned char*)vc + kvoff), 0, 3 * US + 8192, 0x00020000);
  const __amdgpu_buffer_rsrc_t wsR = __builtin_amdgcn_make_buffer_rsrc((void*)ws, 0, WS_END, 0x00020000);

  v4u K[4][2][4];
  v4u V[4][8];
  const int voK = (4 * (c & 3) + ((c >> 2) & 1)) * 256 + (c >> 3) * 64 + g * 16;
  const int voV = lane * 16;
#define LOADK(T, t) do { _Pragma("unroll") for (int p_ = 0; p_ < 4; ++p_) K[T][t][p_] = __builtin_amdgcn_raw_buffer_load_b128(kR, voK + (p_ & 1) * 128 + (p_ >> 1) * 512, (T) * US + (t) * 4096, AUX_NT); SB(); } while (0)
#define LOADV(T, t) do { _Pragma("unroll") for (int j_ = 0; j_ < 4; ++j_) V[T][(t) * 4 + j_] = __builtin_amdgcn_raw_buffer_load_b128(vR, voV + j_ * 1024, (T) * US + (t) * 4096, AUX_NT); SB(); } while (0)

  v4u wqv[6];
  v3u xv[2];
  {
    const __amdgpu_buffer_rsrc_t wqR = __builtin_amdgcn_make_buffer_rsrc((void*)((const unsigned char*)wq + wq_off), 0, (8 * nr - 1) * 8192 + 128, 0x00020000);
    const __amdgpu_buffer_rsrc_t xR = __builtin_amdgcn_make_buffer_rsrc((void*)x, 0, 4096, 0x00020000);
    wqv[0] = __builtin_amdgcn_raw_buffer_load_b128(wqR, vo, 0, AUX_NT);
    wqv[1] = __builtin_amdgcn_raw_buffer_load_b128(wqR, vo, 8192, AUX_NT);
    if (b == 0) {
#pragma unroll
      for (int i = 2; i < 6; ++i) wqv[i] = __builtin_amdgcn_raw_buffer_load_b128(wqR, vo, i * 8192, AUX_NT);
      xv[0] = __builtin_amdgcn_raw_buffer_load_b96(xR, xo, 0, AUX_NT);
      xv[1] = __builtin_amdgcn_raw_buffer_load_b96(xR, 2048 + xo, 0, AUX_NT);
    } else {
#pragma unroll
      for (int i = 2; i < 6; ++i) wqv[i] = v4u{0u, 0u, 0u, 0u};
      xv[0] = v3u{__builtin_amdgcn_raw_buffer_load_b32(xR, xo, 0, AUX_NT), 0u, 0u};
      xv[1] = v3u{__builtin_amdgcn_raw_buffer_load_b32(xR, 2048 + xo, 0, AUX_NT), 0u, 0u};
    }
  }
  const v2u ep = __builtin_amdgcn_raw_buffer_load_b64(wsR, WS_EPOCH, 0, AUX_SC1);
  SB();
  __builtin_amdgcn_s_barrier();
  SB();
  LOADK(0, 0); LOADK(0, 1);
  {
    float qa[2][8];
#pragma unroll
    for (int e = 0; e < 8; ++e) {
      const unsigned sel = (e & 1) ? 0x07060302u : 0x05040100u;
      const unsigned p01 = __builtin_amdgcn_perm(wqv[1][e >> 1], wqv[0][e >> 1], sel);
      const unsigned p23 = __builtin_amdgcn_perm(wqv[3][e >> 1], wqv[2][e >> 1], sel);
      const unsigned p45 = __builtin_amdgcn_perm(wqv[5][e >> 1], wqv[4][e >> 1], sel);
#pragma unroll
      for (int bb = 0; bb < 2; ++bb) {
        float t = dot2(p45, xv[bb].z, dot2(p23, xv[bb].y, dot2(p01, xv[bb].x, 0.f)));
        t += dpp<ROR8>(t);
        qa[bb][e] = x32_sum(x16_sum(t));
      }
    }
    if (lane < 8) {
      float* dst = lds + L_QR + wv * 128 + lane * 8;
#pragma unroll
      for (int bb = 0; bb < 2; ++bb) {
        *(f32x4*)(dst + bb * 64) = f32x4{qa[bb][0], qa[bb][1], qa[bb][2], qa[bb][3]};
        *(f32x4*)(dst + bb * 64 + 4) = f32x4{qa[bb][4], qa[bb][5], qa[bb][6], qa[bb][7]};
      }
    }
    SB();
  }
  const unsigned long long tag = (((unsigned long long)__builtin_amdgcn_readfirstlane(ep.y) << 32) | __builtin_amdgcn_readfirstlane(ep.x)) + 1ull;
  const unsigned t32 = (unsigned)tag;
  const unsigned t40_lo = (unsigned)tag & 0xFFu, t40_hi = (unsigned)(tag >> 8);
  const unsigned t48_lo = (unsigned)tag & 0xFFFFu, t48_hi = (unsigned)(tag >> 16);
  const unsigned long long tw = (tag & 0xFFFFFFFFFFFFull) * 0x001001001001ull;
  const v4u t12 = {(unsigned)tag & 0xFFFu, (unsigned)(tw >> 12) & 0xFFFu, (unsigned)(tw >> 24) & 0xFFFu, (unsigned)(tw >> 36) & 0xFFFu};
  if (tid == 0) { lds_word_store(lds + L_T, 0u); lds_word_store(lds + L_T + 1, 0u); }
  LDS_BARRIER();
  if (tid < 128) {
    float s0 = 0.f;
#pragma unroll
    for (int w = 0; w < 8; ++w) s0 += lds[L_QR + w * 128 + tid];
    const unsigned off = WS_QP + (((G * 16 + cb) * 4 + ((tid >> 4) & 3)) * 4 + kq) * 256 + (tid >> 6) * 128 + (tid & 15) * 8;
    __builtin_amdgcn_raw_buffer_store_b64(v2u{(__float_as_uint(s0) & 0xFFFFFF00u) | t40_lo, t40_hi}, wsR, off, 0, AUX_SC1);
  }
  SB();
  LOADK(1, 0);
  STEP();
  LOADK(1, 1);
  STEP();
  const unsigned off_qp = WS_QP + ((G * 16 + cb) * 4 + kq) * 1024 + lane * 16;
  v4u qp = {0u, 0u, 0u, 0u};
  const bool q_early = wv == 0 && !b, q_late = wv == 0 && b;
  if (q_early) qp = LD128(off_qp);
  SB();
  LOADK(2, 0);
  STEP();
  if (q_late) qp = LD128(off_qp);
  SB();
  LOADK(2, 1);
  STEP();
  LOADK(3, 0);
#define QP_BAD(v) ((((v).x ^ t40_lo) & 0xFFu) | ((v).y ^ t40_hi) | (((v).z ^ t40_lo) & 0xFFu) | ((v).w ^ t40_hi))
#define HOP2() do { \
    if (!__all(QP_BAD(qp) == 0u)) { \
      unsigned spins = 0; \
      do { __builtin_amdgcn_s_sleep(4); RELOAD(); qp = LD128(off_qp); } while (!__all(QP_BAD(qp) == 0u) && ++spins < SPIN_CAP); \
    } \
    const float q0 = x32_sum(x16_sum(__uint_as_float(qp.x & 0xFFFFFF00u))), q1 = x32_sum(x16_sum(__uint_as_float(qp.z & 0xFFFFFF00u))); \
    const unsigned pk = cvtpk(q0, q1);                   \
    if (lane < 16) __builtin_amdgcn_raw_buffer_store_b128(v4u{(pk & 0xFFFFu) | (t48_lo << 16), t48_hi, (pk >> 16) | (t48_lo << 16), t48_hi}, wsR, \
                                             WS_Q + ((lane >> 3) * 4096 + G * 1024 + (cb * 4 + kq) * 16 + (lane & 7) * 2) * 8, 0, AUX_SC1); \
    SB(); } while (0)
  if (q_early) HOP2();
  STEP();
  LOADK(3, 1);
  if (q_late) HOP2();
  STEP();
  LOADV(0, 0);
  STEP();
  const unsigned off_q = WS_Q + (b * 4096 + G * 1024 + ((wv - 1) & 3) * 256) * 8 + lane * 16;
  v4u qg[2] = {v4u{0u, 0u, 0u, 0u}, v4u{0u, 0u, 0u, 0u}};
  if (wv >= 1 && wv <= 4) { qg[0] = LD128(off_q); qg[1] = LD128(off_q + 1024); }
  SB();
  LOADV(0, 1);
  STEP();
  LOADV(1, 0);
  STEP();
  LOADV(1, 1);
#define Q_BAD(v) ((((v).x >> 16) ^ t48_lo) | ((v).y ^ t48_hi) | (((v).z >> 16) ^ t48_lo) | ((v).w ^ t48_hi))
  if (wv >= 1 && wv <= 4) {
    if (!__all((Q_BAD(qg[0]) | Q_BAD(qg[1])) == 0u)) {
      unsigned spins = 0;
      do {
        __builtin_amdgcn_s_sleep(4); RELOAD();
        qg[0] = LD128(off_q); qg[1] = LD128(off_q + 1024);
      } while (!__all((Q_BAD(qg[0]) | Q_BAD(qg[1])) == 0u) && ++spins < SPIN_CAP);
    }
    ((unsigned*)lds)[L_Q + (wv - 1) * 128 + lane] = (qg[0].x & 0xFFFFu) | (qg[0].z << 16);
    ((unsigned*)lds)[L_Q + (wv - 1) * 128 + 64 + lane] = (qg[1].x & 0xFFFFu) | (qg[1].z << 16);
  }
  LDS_BARRIER();
  v4u Q[2];
#pragma unroll
  for (int h = 0; h < 2; ++h) Q[h] = *(const v4u*)(lds + L_Q + (c & 7) * 64 + h * 32 + (c >> 3) * 16 + g * 4);
  f32x4 S[4][2];
#define QK(T) do { _Pragma("unroll") for (int t_ = 0; t_ < 2; ++t_) { const f32x4 z_ = {0.f, 0.f, 0.f, 0.f}; \
    const f32x4 dx_ = mfma(K[T][t_][1], Q[1], mfma(K[T][t_][0], Q[0], z_)), dy_ = mfma(K[T][t_][3], Q[1], mfma(K[T][t_][2], Q[0], z_)); \
    _Pragma("unroll") for (int i_ = 0; i_ < 4; ++i_) { \
      auto sw_ = __builtin_amdgcn_permlane32_swap(__float_as_uint(dx_[i_]), __float_as_uint(dy_[i_]), false, false); \
      S[T][t_][i_] = __uint_as_float(sw_[0]) + dpp<ROR8>(__uint_as_float(sw_[1])); } } SB(); } while (0)
  QK(0); QK(1); QK(2); QK(3);
  constexpr float C2 = 0.08838834764831845f * 1.4426950408889634f;
  float mx = -INFINITY;
#pragma unroll
  for (int T = 0; T < 4; ++T)
#pragma unroll
    for (int t = 0; t < 2; ++t) mx = fmaxf(mx, fmaxf(fmaxf(S[T][t][0], S[T][t][1]), fmaxf(S[T][t][2], S[T][t][3])));
  mx = x32_max(x16_max(mx)) * C2;
  if (lane < 8) lds[L_MAX + lane * 8 + wv] = mx;
  STEP_LDS();
  LOADV(2, 0);
  float M;
  {
    const f32x4 m0 = *(const f32x4*)(lds + L_MAX + (c & 7) * 8), m1 = *(const f32x4*)(lds + L_MAX + (c & 7) * 8 + 4);
    M = fmaxf(fmaxf(fmaxf(m0[0], m0[1]), fmaxf(m0[2], m0[3])), fmaxf(fmaxf(m1[0], m1[1]), fmaxf(m1[2], m1[3])));
    if (c >= 8) M = mx;
  }
  v4u P[4];
  {
    const float msub = (M == -INFINITY) ? 0.f : M;
    float lsum = 0.f;
#pragma unroll
    for (int T = 0; T < 4; ++T) {
      float p[8];
#pragma unroll
      for (int j = 0; j < 8; ++j) { p[j] = __builtin_amdgcn_exp2f(fmaf(S[T][j >> 2][j & 3], C2, -msub)); lsum += p[j]; }
      P[T] = v4u{cvtpk(p[0], p[1]), cvtpk(p[2], p[3]), cvtpk(p[4], p[5]), cvtpk(p[6], p[7])};
    }
    lsum = x32_sum(x16_sum(lsum));
    if (lane < 8) lds[L_SUM + lane * 8 + wv] = lsum;
  }
  LDS_BARRIER();
  if (tid < 16) {
    const f32x4 l0 = *(const f32x4*)(lds + L_SUM + (lane & 7) * 8), l1 = *(const f32x4*)(lds + L_SUM + (lane & 7) * 8 + 4);
    const float L = ((l0[0] + l0[1]) + (l0[2] + l0[3])) + ((l1[0] + l1[1]) + (l1[2] + l1[3]));
    const unsigned u = __float_as_uint(lane < 8 ? M : L);
    __builtin_amdgcn_raw_buffer_store_b128(v4u{(u & 0xFFFFu) | (t48_lo << 16), t48_hi, (u >> 16) | (t48_lo << 16), t48_hi}, wsR,
                                           WS_ML + ((b * 4 + G) * 32 + sp) * 256 + (lane & 7) * 32 + (lane >> 3) * 16, 0, AUX_SC1);
  }
  SB();
  STEP();
  LOADV(2, 1);
  f32x4 O[8];
#pragma unroll
  for (int e = 0; e < 8; ++e) O[e] = f32x4{0.f, 0.f, 0.f, 0.f};
#define PV(T) do { _Pragma("unroll") for (int e_ = 0; e_ < 8; ++e_) { \
      const unsigned sel_ = (e_ & 1) ? 0x07060302u : 0x05040100u; v4u a_; \
      _Pragma("unroll") for (int d_ = 0; d_ < 4; ++d_) a_[d_] = __builtin_amdgcn_perm(V[T][2 * d_ + 1][e_ >> 1], V[T][2 * d_][e_ >> 1], sel_); \
      O[e_] = mfma(a_, P[T], O[e_]); } SB(); } while (0)
#define PV16(t) do { _Pragma("unroll") for (int e_ = 0; e_ < 8; ++e_) { const unsigned sel_ = (e_ & 1) ? 0x07060302u : 0x05040100u; \
      O[e_] = mfma16(v2u{__builtin_amdgcn_perm(V[3][4 * (t) + 1][e_ >> 1], V[3][4 * (t)][e_ >> 1], sel_), __builtin_amdgcn_perm(V[3][4 * (t) + 3][e_ >> 1], V[3][4 * (t) + 2][e_ >> 1], sel_)}, \
                      v2u{P[3][2 * (t)], P[3][2 * (t) + 1]}, O[e_]); } SB(); } while (0)
  PV(0);
  STEP();
  LOADV(3, 0);
  PV(1);
  STEP();
  LOADV(3, 1);
  STEP();
  PV(2);
  const unsigned off_fl = WS_FL + (G * 64 + idx) * 128;
  if (wv == 7) VMWAIT(4);
  SB();
  if (tid == 448) __builtin_amdgcn_raw_buffer_store_b64(v2u{now32(), t32}, wsR, off_fl, 0, AUX_SC1);
  SB();
  PV16(0);
  if (wv == 7) VMWAIT(1);
  asm volatile("" :: "v"(V[3][7]));
  SB();
  if (tid == 448) __builtin_amdgcn_raw_buffer_store_b64(v2u{now32(), t32}, wsR, off_fl + 8, 0, AUX_SC1);
  SB();
  PV16(1);
  const unsigned off_fg = WS_FL + (G * 64 + lane) * 128;
  v4u fl = {0u, 0u, 0u, 0u};
  if (wv == 0) fl = LD128(off_fg);
  if (c < 8) {
    float* dst = lds + L_RED + (wv * 8 + c) * RED_STRIDE + 32 * g;
#pragma unroll
    for (int i = 0; i < 4; ++i) {
      *(f32x4*)(dst + 8 * i) = f32x4{O[0][i], O[1][i], O[2][i], O[3][i]};
      *(f32x4*)(dst + 8 * i + 4) = f32x4{O[4][i], O[5][i], O[6][i], O[7][i]};
    }
  }
  const unsigned off_mg = WS_ML + (((wv >> 1) * 4 + G) * 32 + (lane >> 2)) * 256 + hB * 32 + (lane & 1) * 16;
  v4u ml[2] = {v4u{0u, 0u, 0u, 0u}, v4u{0u, 0u, 0u, 0u}};
  if (wv < 4) { ml[0] = LD128(off_mg); ml[1] = LD128(off_mg + 4096); }
  LDS_BARRIER();
  if (wv >= 4) {
    const int h = (tid >> 5) & 7, d4 = tid & 31;
    f32x4 a = *(const f32x4*)(lds + L_RED + h * RED_STRIDE + 4 * d4);
#pragma unroll
    for (int w = 1; w < 8; ++w) a += *(const f32x4*)(lds + L_RED + (w * 8 + h) * RED_STRIDE + 4 * d4);
    __builtin_amdgcn_raw_buffer_store_b128(tag4(a, t12), wsR, WS_PO + ((G * 2 + b) * 32 + sp) * 4096 + (tid & 255) * 16, 0, AUX_SC1);
  }
#define FL_BAD (fl.y != t32 && fl.w != t32)
  if (wv == 0) {
    unsigned spins = 0;
    while (__any(FL_BAD && (lane >> 5) == b) && ++spins < SPIN_CAP) {
      __builtin_amdgcn_s_sleep(4); RELOAD();
      if (FL_BAD) fl = LD128(off_fg);
    }
    if (lane == 0) lds_word_store(lds + L_T + 1, 1u);
  } else {
    while (lds_word_load(lds + L_T + 1) == 0u) __builtin_amdgcn_s_sleep(1);
  }
  v4u wov[4];
  {
    const __amdgpu_buffer_rsrc_t woR = __builtin_amdgcn_make_buffer_rsrc((void*)(wo + (size_t)((G * 8 + hB) * 128 + dq * 32) * 1024 + jh * 512 + wv * 64), 0, 31 * 2048 + 128, 0x00020000);
#pragma unroll
    for (int i = 0; i < 4; ++i) wov[i] = __builtin_amdgcn_raw_buffer_load_b128(woR, (lane >> 3) * 2048 + (lane & 7) * 16, i * 16384, AUX_NT);
    SB();
  }
  v4u WA[4];
#define STAGE_WO() do { float* wt = lds + L_WOT + wv * 32 * WOT_STRIDE; \
    _Pragma("unroll") for (int i = 0; i < 4; ++i) *(v4u*)(wt + (8 * i + (lane >> 3)) * WOT_STRIDE + (lane & 7) * 4) = wov[i]; \
    _Pragma("unroll") for (int u = 0; u < 4; ++u) { \
      const float* p0 = wt + (8 * g + (c >> 2)) * WOT_STRIDE + u * 8 + (c & 3) * 2; \
      const v2u lo = tr_read(p0), hi = tr_read(p0 + 4 * WOT_STRIDE); \
      WA[u] = v4u{lo.x, lo.y, hi.x, hi.y}; } SB(); } while (0)
#define LD_FA(k) __builtin_amdgcn_raw_buffer_load_b64(wsR, WS_FL + 8 + lane * 128, (k) * 8192, AUX_SC1)
  v2u fa[4];
  if (wv >= 4) {
    STAGE_WO();
    if (wv == 4) {
#pragma unroll
      for (int k = 0; k < 4; ++k) fa[k] = LD_FA(k);
      asm volatile("" :: "v"(fa[0]), "v"(fa[1]), "v"(fa[2]), "v"(fa[3]));
    }
  } else {
    float w0, w1;
    {
      if (!__all((Q_BAD(ml[0]) | Q_BAD(ml[1])) == 0u)) {
        unsigned spins = 0;
        do {
          __builtin_amdgcn_s_sleep(4); RELOAD();
          ml[0] = LD128(off_mg); ml[1] = LD128(off_mg + 4096);
        } while (!__all((Q_BAD(ml[0]) | Q_BAD(ml[1])) == 0u) && ++spins < SPIN_CAP);
      }
      const float v0 = __uint_as_float((ml[0].x & 0xFFFFu) | (ml[0].z << 16)), v1 = __uint_as_float((ml[1].x & 0xFFFFu) | (ml[1].z << 16));
      const float M0 = dpp<QUAD0>(v0), L0 = dpp<QUAD1>(v0), M1 = dpp<QUAD0>(v1), L1 = dpp<QUAD1>(v1);
      float Mg = fmaxf(M0, M1); Mg = fmaxf(Mg, dpp<ROR4>(Mg)); Mg = fmaxf(Mg, dpp<ROR8>(Mg)); Mg = x32_max(x16_max(Mg));
      w0 = (L0 > 0.f) ? __builtin_amdgcn_exp2f(M0 - Mg) : 0.f; w1 = (L1 > 0.f) ? __builtin_amdgcn_exp2f(M1 - Mg) : 0.f;
      float Lt = w0 * L0 + w1 * L1; Lt += dpp<ROR4>(Lt); Lt += dpp<ROR8>(Lt); Lt = x32_sum(x16_sum(Lt));
      const float inv = 1.f / Lt;
      w0 *= inv; w1 *= inv;
      SB();
    }
    unsigned spins = 0, t_read;
    if (wv == 0) {
      while (__any(FL_BAD) && ++spins < SPIN_CAP) {
        __builtin_amdgcn_s_sleep(4); RELOAD();
        if (FL_BAD) fl = LD128(off_fg);
      }
      const unsigned ref = now32();
      t_read = (ref + (unsigned)min(wave_max_i32(fl.w == t32 ? (int)(fl.z - ref) + D_PO : (int)(fl.x - ref) + D_PO1), 500)) | 1u;
      if (lane == 0) lds_word_store(lds + L_T, t_read);
      spins = 0;
    } else {
      while ((t_read = lds_word_load(lds + L_T)) == 0u) __builtin_amdgcn_s_sleep(1);
    }
    { const unsigned ref = now32(); wait_until(ref, (int)(t_read - ref)); }
    const unsigned off_pg = WS_PO + ((G * 2 + (wv >> 1)) * 32 + (lane >> 2)) * 4096 + hB * 512 + dq * 128 + (wv & 1) * 64 + (lane & 3) * 16;
    v4u pa[2];
    pa[0] = LD128(off_pg);
    pa[1] = LD128(off_pg + 65536);
    SB();
    STAGE_WO();
    for (;;) {
      const bool bad0 = bad4(pa[0], t12), bad1 = bad4(pa[1], t12);
      if (!__any(bad0 | bad1) || ++spins > SPIN_CAP) break;
      RELOAD();
      if (bad0) pa[0] = LD128(off_pg);
      if (bad1) pa[1] = LD128(off_pg + 65536);
    }
    float o[4];
#pragma unroll
    for (int i = 0; i < 4; ++i) {
      float t = w0 * val(pa[0][i]) + w1 * val(pa[1][i]);
      t += dpp<ROR4>(t); t += dpp<ROR8>(t);
      o[i] = x32_sum(x16_sum(t));
    }
    if (lane < 4) *(v2u*)(lds + L_OB + (wv >> 1) * 16 + (wv & 1) * 8 + lane * 2) = v2u{cvtpk(o[0], o[1]), cvtpk(o[2], o[3])};
  }
#define FA_BAD (((fa[0].y ^ t32) | (fa[1].y ^ t32) | (fa[2].y ^ t32) | (fa[3].y ^ t32)) != 0u)
#define FA_MAX wave_max_i32(max(max((int)(fa[0].x - fa_ref), (int)(fa[1].x - fa_ref)), max((int)(fa[2].x - fa_ref), (int)(fa[3].x - fa_ref))))
  int fa_max = 0; bool fa_all = false; unsigned fa_ref = 0;
  if (wv == 4) {
    fa_all = !__any(FA_BAD);
    fa_ref = now32();
    fa_max = FA_MAX;
  }
  LDS_BARRIER();
  {
    const v4u ob = *(const v4u*)(lds + L_OB + (c & 1) * 16 + g * 4);
    f32x4 d = {0.f, 0.f, 0.f, 0.f};
#pragma unroll
    for (int u = 0; u < 4; ++u) {
      const bool on = (c >> 1) == u;
      d = mfma(WA[u], v4u{on ? ob.x : 0u, on ? ob.y : 0u, on ? ob.z : 0u, on ? ob.w : 0u}, d);
    }
    const unsigned off_f = WS_F + ((jh * 32 + wv * 4 + ((c >> 1) & 3)) * 128 + (G * 8 + hB) * 4 + dq) * 128 + (c & 1) * 64 + g * 16;
    if (c < 8) __builtin_amdgcn_raw_buffer_store_b128(tag4(d, t12), wsR, off_f, 0, AUX_SC1);
  }
  if (wv == 4) {
    unsigned spins = 0;
    if (!fa_all) {
      do {
        __builtin_amdgcn_s_sleep(4); RELOAD();
#pragma unroll
        for (int k = 0; k < 4; ++k) if (fa[k].y != t32) fa[k] = LD_FA(k);
      } while (__any(FA_BAD) && ++spins < SPIN_CAP);
      fa_ref = now32();
      fa_max = FA_MAX;
    }
    wait_until(fa_ref, fa_max + D_F);
    const int cq = xcd * 32 + sp;
    const unsigned off = WS_F + (cq >> 2) * 16384 + (lane >> 1) * 128 + (lane & 1) * 64 + (cq & 3) * 16;
    v4u f[4];
#pragma unroll
    for (int k = 0; k < 4; ++k) f[k] = LD128(off + k * 4096);
    spins = 0;
    for (;;) {
      bool bad[4];
#pragma unroll
      for (int k = 0; k < 4; ++k) bad[k] = bad4(f[k], t12);
      if (!__any(bad[0] | bad[1] | bad[2] | bad[3]) || ++spins > SPIN_CAP) break;
      RELOAD();
#pragma unroll
      for (int k = 0; k < 4; ++k) if (bad[k]) f[k] = LD128(off + k * 4096);
    }
    float s[4];
#pragma unroll
    for (int i = 0; i < 4; ++i) {
      float t = (val(f[0][i]) + val(f[1][i])) + (val(f[2][i]) + val(f[3][i]));
      t += dpp<ROR2>(t); t += dpp<ROR4>(t); t += dpp<ROR8>(t);
      s[i] = x32_sum(x16_sum(t));
    }
    if (lane < 2) *(v2u*)(out + lane * 1024 + cq * 4) = v2u{cvtpk(s[0], s[1]), cvtpk(s[2], s[3])};
    if (wg == 0 && lane == 0) __builtin_amdgcn_raw_buffer_store_b64(v2u{(unsigned)tag, (unsigned)(tag >> 32)}, wsR, WS_EPOCH, 0, AUX_SC1);
  }
}

extern "C" void kernel_launch(void* const* d_in, const int* in_sizes, int n_in,
                              void* d_out, int out_size, void* d_ws, size_t ws_size,
                              hipStream_t stream) {
  (void)in_sizes; (void)n_in; (void)out_size; (void)ws_size;
  gqa_decode_fused<<<NWG, NT, 0, stream>>>((const u16*)d_in[0], (const u16*)d_in[1], (const u16*)d_in[2],
                                           (const u16*)d_in[3], (const u16*)d_in[4], (u16*)d_out, (unsigned char*)d_ws);
}
```
